```python
import torch

def _softmax(x, axis=-1):
    return torch.softmax(x, dim=axis)

def _gelu(x):
    return torch.nn.functional.gelu(x, approximate='tanh')

def _silu(x):
    return torch.nn.functional.silu(x)

def _sigmoid(x):
    return torch.sigmoid(x)

def _layernorm(x, axis=-1, eps=1e-05):
    m = x.mean(dim=axis, keepdim=True)
    v = x.var(dim=axis, keepdim=True, unbiased=False)
    return (x - m) / torch.sqrt(v + eps)

def _scatter_add(tgt, idx, src, axis=0):
    return tgt.clone().index_add_(axis, idx, src)

def _segment_sum(src, seg, n):
    out = torch.zeros((n,) + src.shape[1:], dtype=src.dtype)
    return out.index_add_(0, seg, src)

def _pad(x, pw):
    flat = tuple((v for pair in reversed(pw) for v in pair))
    return torch.nn.functional.pad(x, flat)
kernel_name = 'adaLN_cond__mha_block__moe_arch_mod__prompt'

def setup_inputs():

    _w = lambda a: (lambda t: t.to(torch.bfloat16) if t.is_floating_point() else t)(torch.as_tensor(a))

    def _A_setup():
        B, S, D, H = (1, 2048, 512, 32)
        return {'x': _w((lambda *s: torch.randn(*s, dtype=torch.bfloat16) * 0.05)(B, S, D)), 'wqkv': _w((lambda *s: torch.randn(*s, dtype=torch.bfloat16) * 0.05)(D, 3 * D)), 'wo': _w((lambda *s: torch.randn(*s, dtype=torch.bfloat16) * 0.05)(D, D))}
    _A_in = _A_setup()

    def _B_setup():
        B, S, D, F, C = (1, 2048, 512, 1792, 2)
        return {'x': _w((lambda *s: torch.randn(*s, dtype=torch.bfloat16) * 0.05)(B, S, D)), 'w_r': _w((lambda *s: torch.randn(*s, dtype=torch.bfloat16) * 0.05)(D)), 'w_gate': _w((lambda *s: torch.randn(*s, dtype=torch.bfloat16) * 0.05)(D, F)), 'w_up': _w((lambda *s: torch.randn(*s, dtype=torch.bfloat16) * 0.05)(D, F)), 'w_down': _w((lambda *s: torch.randn(*s, dtype=torch.bfloat16) * 0.05)(F, D))}
    _B_in = _B_setup()
    return {'A_x': _A_in['x'], 'A_wqkv': _A_in['wqkv'], 'A_wo': _A_in['wo'], 'B_x': _B_in['x'], 'B_w_r': _B_in['w_r'], 'B_w_gate': _B_in['w_gate'], 'B_w_up': _B_in['w_up'], 'B_w_down': _B_in['w_down'], 'c': _w((lambda *s: torch.randn(*s, dtype=torch.bfloat16) * 0.1)(1, 512) * 8.0), 'w_mod': _w((lambda *s: torch.randn(*s, dtype=torch.bfloat16) * 0.1)(512, 6 * 512) * 512 ** (-0.5) * 8.0)}

def reference(A_x, A_wqkv, A_wo, B_x, B_w_r, B_w_gate, B_w_up, B_w_down, c, w_mod):

    def _A_ref(x, wqkv, wo):
        B, S, D = x.shape
        H = 32
        Dh = D // H
        qkv = (lambda x, *d: x.permute(*d))((x @ wqkv).reshape(B, S, 3, H, Dh), 2, 0, 3, 1, 4)
        q, k, v = (qkv[0], qkv[1], qkv[2])
        scores = torch.einsum('bhqd,bhkd->bhqk', q, k) * Dh ** (-0.5)
        p = _softmax(scores, axis=-1)
        o = torch.einsum('bhqk,bhkd->bhqd', p, v)
        return (lambda x, *d: x.permute(*d))(o, 0, 2, 1, 3).reshape(B, S, D) @ wo

    def _B_ref(x, w_r, w_gate, w_up, w_down):
        B, S, D = x.shape
        C = 2
        r = x @ w_r
        idx = torch.argsort(-r, axis=-1)[:, :C]
        idx_e = (lambda x, n, axis: torch.repeat_interleave(x, n, dim=axis))(idx.reshape(B, C, 1), D, axis=2)
        x_sel = (lambda x, i, axis: torch.take_along_dim(x, i, dim=axis))(x, idx_e, axis=1)
        g = _silu(x_sel @ w_gate)
        u = x_sel @ w_up
        y = g * u @ w_down
        r_sel = _sigmoid((lambda x, i, axis: torch.take_along_dim(x, i, dim=axis))(r, idx, axis=1))
        y = y * r_sel.reshape(B, C, 1)
        b_off = (torch.arange(B) * S).reshape(B, 1)
        flat_idx = (idx + b_off).reshape(B * C)
        zero = (x * 0.0).reshape(B * S, D)
        upd = _scatter_add(zero, flat_idx, y.reshape(B * C, D), axis=0)
        return x + upd.reshape(B, S, D)
    x = A_x
    mod = (_silu(c) @ w_mod).reshape(c.shape[0], 6, 1, -1)
    sa, ca, ga, sb, cb, gb = (mod[:, 0].clone(), mod[:, 1].clone(), mod[:, 2].clone(), mod[:, 3].clone(), mod[:, 4].clone(), mod[:, 5].clone())
    h = _layernorm(x, axis=-1) * (1 + ca) + sa
    x = x + ga * _A_ref(h, A_wqkv, A_wo)
    h = _layernorm(x, axis=-1) * (1 + cb) + sb
    x = x + gb * _B_ref(h, B_w_r, B_w_gate, B_w_up, B_w_down)
    return x
```

```cpp
#include <hip/hip_runtime.h>
#include <hip/hip_bf16.h>
#include <stdint.h>

typedef unsigned short u16;
__device__ __forceinline__ float bf2f(u16 v) { return __uint_as_float(((unsigned)v) << 16); }
__device__ __forceinline__ u16 f2bf(float f) { unsigned u = __float_as_uint(f); u += 0x7fffu + ((u >> 16) & 1u); return (u16)(u >> 16); }

constexpr int S = 2048, D = 512, F = 1792, H = 32, DH = 16;

__global__ void k_mod(const u16* c, const u16* wmod, float* mod) {
    int j = blockIdx.x * blockDim.x + threadIdx.x;
    float acc = 0.f;
    for (int k = 0; k < D; ++k) { float cv = bf2f(c[k]); float s = cv / (1.f + __expf(-cv)); acc += s * bf2f(wmod[k * 3072 + j]); }
    mod[j] = acc;
}
__global__ void k_ln(const float* xin_f, const u16* xin_b, const float* mod, int shift_off, int scale_off, float* h) {
    int row = blockIdx.x; int t = threadIdx.x;
    __shared__ float red[16];
    float v = xin_f ? xin_f[row * D + t] : bf2f(xin_b[row * D + t]);
    float s = v; for (int o = 32; o > 0; o >>= 1) s += __shfl_xor(s, o);
    if ((t & 63) == 0) red[t >> 6] = s; __syncthreads();
    float m = 0; for (int i = 0; i < 8; ++i) m += red[i]; m /= D; __syncthreads();
    float d = v - m; float q = d * d; for (int o = 32; o > 0; o >>= 1) q += __shfl_xor(q, o);
    if ((t & 63) == 0) red[t >> 6] = q; __syncthreads();
    float var = 0; for (int i = 0; i < 8; ++i) var += red[i]; var /= D;
    h[row * D + t] = d / sqrtf(var + 1e-5f) * (1.f + mod[scale_off + t]) + mod[shift_off + t];
}
__global__ void k_gemm(const float* A, const u16* B, float* C, int M, int N, int K) {
    int n = blockIdx.x * 64 + (threadIdx.x & 63); int m0 = blockIdx.y * 16 + (threadIdx.x >> 6) * 4;
    float acc[4] = {0, 0, 0, 0};
    for (int k = 0; k < K; ++k) { float b = bf2f(B[k * N + n]); for (int i = 0; i < 4; ++i) acc[i] += A[(m0 + i) * K + k] * b; }
    for (int i = 0; i < 4; ++i) C[(m0 + i) * N + n] = acc[i];
}
__global__ void k_attn(const float* qkv, float* o) {
    int h = blockIdx.y; int q = blockIdx.x * 256 + threadIdx.x;
    float qv[DH]; for (int d = 0; d < DH; ++d) qv[d] = qkv[q * 1536 + h * DH + d] * 0.25f;
    float m = -1e30f, l = 0, acc[DH]; for (int d = 0; d < DH; ++d) acc[d] = 0;
    for (int j = 0; j < S; ++j) {
        float s = 0; for (int d = 0; d < DH; ++d) s += qv[d] * qkv[j * 1536 + 512 + h * DH + d];
        float mn = fmaxf(m, s); float a = __expf(m - mn); float p = __expf(s - mn);
        l = l * a + p; for (int d = 0; d < DH; ++d) acc[d] = acc[d] * a + p * qkv[j * 1536 + 1024 + h * DH + d];
        m = mn;
    }
    for (int d = 0; d < DH; ++d) o[q * D + h * DH + d] = acc[d] / l;
}
__global__ void k_res(const u16* x, const float* proj, const float* mod, float* x2) {
    int i = blockIdx.x * blockDim.x + threadIdx.x; x2[i] = bf2f(x[i]) + mod[2 * D + (i & (D - 1))] * proj[i];
}
__global__ void k_r(const float* h2, const u16* wr, float* r) {
    int row = blockIdx.x; int t = threadIdx.x; float s = 0;
    for (int k = t; k < D; k += 64) s += h2[row * D + k] * bf2f(wr[k]);
    for (int o = 32; o > 0; o >>= 1) s += __shfl_xor(s, o);
    if (t == 0) r[row] = s;
}
__global__ void k_top2(const float* r, int* idx) {
    if (threadIdx.x == 0) { int b1 = 0; for (int i = 1; i < S; ++i) if (r[i] > r[b1]) b1 = i;
        int b2 = b1 == 0 ? 1 : 0; for (int i = 0; i < S; ++i) if (i != b1 && r[i] > r[b2]) b2 = i; idx[0] = b1; idx[1] = b2; }
}
__global__ void k_gu(const float* h2, const int* idx, const u16* wg, const u16* wu, float* act) {
    int f = blockIdx.x * blockDim.x + threadIdx.x; int t = blockIdx.y; const float* x = h2 + idx[t] * D;
    float g = 0, u = 0; for (int k = 0; k < D; ++k) { g += x[k] * bf2f(wg[k * F + f]); u += x[k] * bf2f(wu[k * F + f]); }
    act[t * F + f] = g / (1.f + __expf(-g)) * u;
}
__global__ void k_out(const float* x2, const float* h2, const float* mod, u16* out) {
    int i = blockIdx.x * blockDim.x + threadIdx.x; out[i] = f2bf(x2[i] + mod[5 * D + (i & (D - 1))] * h2[i]);
}
__global__ void k_down(const float* act, const u16* wd, const int* idx, const float* r, const float* x2, const float* h2, const float* mod, u16* out) {
    int n = blockIdx.x * blockDim.x + threadIdx.x; int t = blockIdx.y; int row = idx[t];
    float y = 0; for (int f = 0; f < F; ++f) y += act[t * F + f] * bf2f(wd[f * D + n]);
    float sg = 1.f / (1.f + __expf(-r[row]));
    out[row * D + n] = f2bf(x2[row * D + n] + mod[5 * D + n] * (h2[row * D + n] + y * sg));
}

extern "C" void kernel_launch(void* const* d_in, const int* in_sizes, int n_in, void* d_out, int out_size, void* d_ws, size_t ws_size, hipStream_t stream) {
    const u16* x = (const u16*)d_in[0]; const u16* wqkv = (const u16*)d_in[1]; const u16* wo = (const u16*)d_in[2];
    const u16* wr = (const u16*)d_in[4]; const u16* wg = (const u16*)d_in[5]; const u16* wu = (const u16*)d_in[6]; const u16* wd = (const u16*)d_in[7];
    const u16* c = (const u16*)d_in[8]; const u16* wmod = (const u16*)d_in[9];
    float* ws = (float*)d_ws;
    float* mod = ws; float* h = mod + 4096; float* qkv = h + S * D; float* o = qkv + S * 1536; float* proj = o + S * D; float* x2 = proj + S * D; float* h2 = x2 + S * D;
    float* r = h2 + S * D; int* idx = (int*)(r + S); float* act = r + S + 64;
    k_mod<<<12, 256, 0, stream>>>(c, wmod, mod);
    k_ln<<<S, 512, 0, stream>>>(nullptr, x, mod, 0, D, h);
    k_gemm<<<dim3(1536 / 64, S / 16), 256, 0, stream>>>(h, wqkv, qkv, S, 1536, D);
    k_attn<<<dim3(S / 256, H), 256, 0, stream>>>(qkv, o);
    k_gemm<<<dim3(D / 64, S / 16), 256, 0, stream>>>(o, wo, proj, S, D, D);
    k_res<<<S * D / 256, 256, 0, stream>>>(x, proj, mod, x2);
    k_ln<<<S, 512, 0, stream>>>(x2, nullptr, mod, 3 * D, 4 * D, h2);
    k_r<<<S, 64, 0, stream>>>(h2, wr, r);
    k_top2<<<1, 64, 0, stream>>>(r, idx);
    k_out<<<S * D / 256, 256, 0, stream>>>(x2, h2, mod, (u16*)d_out);
    k_gu<<<dim3(F / 256, 2), 256, 0, stream>>>(h2, idx, wg, wu, act);
    k_down<<<dim3(D / 256, 2), 256, 0, stream>>>(act, wd, idx, r, x2, h2, mod, (u16*)d_out);
}
```

```cpp
#include <hip/hip_runtime.h>
#include <hip/hip_bf16.h>
#include <stdint.h>

typedef unsigned short u16;
typedef unsigned int u32;
typedef unsigned long long u64;
typedef float f32x2 __attribute__((ext_vector_type(2)));
typedef float f32x4 __attribute__((ext_vector_type(4)));
typedef float f32x16 __attribute__((ext_vector_type(16)));
typedef float f32x8 __attribute__((ext_vector_type(8)));
typedef u32 u32x2 __attribute__((ext_vector_type(2)));
typedef u32 u32x4 __attribute__((ext_vector_type(4)));
typedef __bf16 bf16x2 __attribute__((ext_vector_type(2)));
typedef __bf16 bf16x8 __attribute__((ext_vector_type(8)));
typedef __bf16 bf16x16 __attribute__((ext_vector_type(16)));
#define GAS __attribute__((address_space(1)))
#define LAS __attribute__((address_space(3)))
typedef GAS u32 gu32;
typedef GAS u64 gu64;
#define DI __device__ __forceinline__
#define RLX __ATOMIC_RELAXED, __HIP_MEMORY_SCOPE_AGENT

constexpr int S = 2048, D = 512, F = 1792;
constexpr u32 WS_EPOCH = 0;
constexpr u32 WS_TMO   = 256;
constexpr u32 WS_MODG  = 4096;
constexpr u32 WS_STATG = WS_MODG + 4 * 3072 * 8;
constexpr u32 WS_CSB   = WS_STATG + 4096 * 8;
constexpr u32 WS_KVF   = WS_CSB + 32 * 96 * 8;
constexpr u32 WS_OF    = WS_KVF + 2048 * 256;
constexpr u32 WS_PST   = WS_OF + 2048 * 64;
constexpr u32 WS_CAND  = WS_PST + 32 * 8 * 192 * 8;
constexpr u32 WS_ACT   = WS_CAND + 256 * 8;
constexpr u32 WS_WTF   = WS_ACT + F * 8;
constexpr u32 WS_MODF  = WS_WTF + 256 * 64;
constexpr u32 WS_KV    = 2u << 20;
constexpr u32 WS_O     = 6u << 20;
constexpr u32 WS_X2G   = 8u << 20;
constexpr u32 WS_WT    = 17u << 19;
constexpr u32 WS_END   = 12u << 20;
static_assert(WS_X2G + 32 * 2 * 512 * 8 <= WS_WT && WS_WT + 32 * 49152 <= (21u << 19) && WS_MODF + 1024 * 8 <= WS_KV, "ws map");
constexpr u32 L_KV = 0, L_ONE = 131072, L_MISC = 135168, L_END = 163840;
constexpr u32 L_XA = 0;
constexpr u32 L_WP = 16384;
constexpr u32 L_XT = L_MISC;
constexpr u32 L_Q = L_MISC;
constexpr u32 L_AS = L_MISC + 16384;
constexpr u32 L_STAT = L_AS + 4096;
constexpr u32 L_WS = L_AS;
constexpr u32 L_CSB = L_STAT + 2048;
constexpr u32 L_RED = L_CSB + 512;
constexpr u32 L_FLAG = L_RED + 1024;
constexpr u32 L_FLAG2 = L_MISC + 28672 - 4096 + 3400;
constexpr u32 L_XCH = L_MISC;
constexpr u32 L_WO = 0, L_OI = 65536;
constexpr u32 L_GU = L_MISC;
constexpr u32 L_MODT = L_MISC + 16384;
constexpr u32 L_T = L_MODT + 8192;
constexpr u32 L_EX = L_OI, L_XR = L_WO;
constexpr u32 L_PS = L_WO + 12288;
constexpr u32 L_TS = L_T + 3200;
constexpr u32 L_GK = L_ONE;
constexpr u32 L_H2 = L_ONE + 2048;
constexpr u32 L_X2S = L_WO;
static_assert(L_T + 4096 <= L_END && L_FLAG + 256 <= L_T && L_XCH + 18432 <= L_FLAG, "lds map");

struct Params {
    const u16* x; const u16* wqkv; const u16* wo; const u16* wr; const u16* wg; const u16* wu; const u16* wd; const u16* c; const u16* wmod;
    u16* out; char* ws;
};

DI float bflo(u32 v) { return __uint_as_float(v << 16); }
DI float bfhi(u32 v) { return __uint_as_float(v & 0xffff0000u); }
DI float bf2f(u16 v) { return __uint_as_float(((u32)v) << 16); }
DI u32 pk2(float lo, float hi) { const bf16x2 v = {(__bf16)lo, (__bf16)hi}; return __builtin_bit_cast(u32, v); }
DI float rbf(float f) { return __uint_as_float(pk2(f, f) << 16); }
DI void bar() { __builtin_amdgcn_sched_barrier(0); asm volatile("s_waitcnt lgkmcnt(0)\n\ts_barrier" ::: "memory"); __builtin_amdgcn_sched_barrier(0); }
DI void SB() { asm volatile("" ::: "memory"); __builtin_amdgcn_sched_barrier(0); }
DI void lds_set(LAS char* p) { asm volatile("s_waitcnt lgkmcnt(0)" ::: "memory"); if ((threadIdx.x & 63) == 0) __hip_atomic_store((LAS u32*)p, 1u, __ATOMIC_RELAXED, __HIP_MEMORY_SCOPE_WORKGROUP); }
template <int N> DI void lds_await(const LAS char* p) {
    const int i = (threadIdx.x & 63) < N ? (threadIdx.x & 63) : 0;
    while (!__all(__hip_atomic_load((const LAS u32*)(p + i * 4), __ATOMIC_RELAXED, __HIP_MEMORY_SCOPE_WORKGROUP) != 0)) __builtin_amdgcn_s_sleep(0);
    asm volatile("" ::: "memory");
}
DI void cfence() { asm volatile("" ::: "memory"); }
DI void drain() { asm volatile("s_waitcnt vmcnt(0)" ::: "memory"); }
DI void put_granule(char* ws, u32 off, u32 tag, u32 val) { __hip_atomic_store((gu64*)(ws + off), ((u64)tag << 32) | val, RLX); }
DI u64 get_granule(char* ws, u32 off) { return __hip_atomic_load((gu64*)(ws + off), RLX); }
DI void put_flag(char* ws, u32 off, u32 tag) { __hip_atomic_store((gu32*)(ws + off), tag, RLX); }
DI void put_flag64(char* ws, u32 off, u32 tag, int lane) { if (lane < 16) __hip_atomic_store((gu32*)(ws + off + lane * 4), tag, RLX); }
DI u32 get_flag(char* ws, u32 off) { return __hip_atomic_load((gu32*)(ws + off), RLX); }
constexpr u32 SPIN_CAP = 1u << 16;
DI bool spin(u32& n, char* ws) {
    __builtin_amdgcn_s_sleep(1);
    if (++n > SPIN_CAP) { put_flag(ws, WS_TMO, 1u); return true; }
    return false;
}
template <int CTRL> DI float dpp_add(float v) { return v + __int_as_float(__builtin_amdgcn_update_dpp(0, __float_as_int(v), CTRL, 0xf, 0xf, true)); }
DI float row16_sum(float v) { v = dpp_add<0x128>(v); v = dpp_add<0x124>(v); v = dpp_add<0x122>(v); v = dpp_add<0x121>(v); return v; }
DI float swap32_sum(float a, float b) { auto r = __builtin_amdgcn_permlane32_swap(__float_as_uint(a), __float_as_uint(b), false, false); return __uint_as_float(r[0]) + __uint_as_float(r[1]); }
DI float swap16_sum(float a, float b) { auto r = __builtin_amdgcn_permlane16_swap(__float_as_uint(a), __float_as_uint(b), false, false); return __uint_as_float(r[0]) + __uint_as_float(r[1]); }
DI float wave_sum(float v) { v = row16_sum(v); v = swap16_sum(v, v); v = swap32_sum(v, v); return v; }
template <int CTRL> DI u32 dpp_max(u32 v) { return max(v, (u32)__builtin_amdgcn_update_dpp(0, (int)v, CTRL, 0xf, 0xf, true)); }
DI u32 wave_max(u32 v) {
    v = dpp_max<0x128>(v); v = dpp_max<0x124>(v); v = dpp_max<0x122>(v); v = dpp_max<0x121>(v);
    auto a = __builtin_amdgcn_permlane16_swap(v, v, false, false); v = max(a[0], a[1]);
    auto c = __builtin_amdgcn_permlane32_swap(v, v, false, false); return max(c[0], c[1]);
}
DI void wave_reduce16(const float (&v)[16], float (&o)[4]) {
    float a[8], b[4];
#pragma unroll
    for (int i = 0; i < 8; ++i) a[i] = swap32_sum(v[i], v[i + 8]);
#pragma unroll
    for (int i = 0; i < 4; ++i) b[i] = swap16_sum(a[i], a[i + 4]);
#pragma unroll
    for (int i = 0; i < 4; ++i) o[i] = row16_sum(b[i]);
}
DI float dot2(u32 a, u32 b, float c) { return __builtin_amdgcn_fdot2_f32_bf16(__builtin_bit_cast(bf16x2, a), __builtin_bit_cast(bf16x2, b), c, false); }
DI float ln_rstd(float var) { return __builtin_amdgcn_rcpf(rbf(__builtin_amdgcn_sqrtf(rbf(rbf(var) + 1e-5f)))); }
DI float silu(float v) { return v * __builtin_amdgcn_rcpf(1.f + __expf(-v)); }
#define MFMA16(a, b, c) __builtin_amdgcn_mfma_f32_16x16x32_bf16(a, b, c, 0, 0, 0)
#define MFMA32(a, b, c) __builtin_amdgcn_mfma_f32_32x32x16_bf16(a, b, c, 0, 0, 0)
typedef short v4i16_t __attribute__((ext_vector_type(4)));
DI u32x2 trd(const LAS char* p) { return __builtin_bit_cast(u32x2, __builtin_amdgcn_ds_read_tr16_b64_v4i16((LAS v4i16_t*)p)); }
DI bf16x8 cat(u32x2 lo, u32x2 hi) { u32x4 t = {lo[0], lo[1], hi[0], hi[1]}; return __builtin_bit_cast(bf16x8, t); }
DI bf16x8 asbf(u32x4 v) { return __builtin_bit_cast(bf16x8, v); }

#define ATTN_LOOP4 \
  "ds_read_b128 v[96:99], %[ka]\n\t" \
  "v_mov_b32 v88, 0\n\t" \
  "v_mov_b32 v89, 0\n\t" \
  "v_mov_b32 v90, 0\n\t" \
  "v_mov_b32 v91, 0\n\t" \
  "v_mov_b32 v92, 0\n\t" \
  "v_mov_b32 v93, 0\n\t" \
  "v_mov_b32 v94, 0\n\t" \
  "v_mov_b32 v95, 0\n\t" \
  "v_mov_b32 v144, 0\n\t" \
  "v_mov_b32 v145, 0\n\t" \
  "v_mov_b32 v146, 0\n\t" \
  "v_mov_b32 v147, 0\n\t" \
  "v_mov_b32 v148, 0\n\t" \
  "v_mov_b32 v149, 0\n\t" \
  "v_mov_b32 v150, 0\n\t" \
  "v_mov_b32 v151, 0\n\t" \
  "s_waitcnt lgkmcnt(0)\n\t" \
  "v_mfma_f32_32x32x16_bf16 v[64:79], v[96:99], %[q], 0\n\t" \
  "v_mfma_f32_32x32x16_bf16 v[120:135], v[96:99], %[q2], 0\n\t" \
  "ds_read_b128 v[108:111], %[va] offset:512\n\t" \
  "ds_read_b128 v[96:99], %[ka] offset:1024\n\t" \
  "ds_read_b128 v[104:107], %[va]\n\t" \
  "s_nop 7\n\t" \
  "s_nop 4\n\t" \
  "1:\n\t" \
  "v_exp_f32 v64, v64\n\t" \
  "v_exp_f32 v65, v65\n\t" \
  "s_waitcnt lgkmcnt(2)\n\t" \
  "v_smfmac_f32_32x32x32_bf16 %[o], v[108:111], v[88:95], %[idx]\n\t" \
  "v_exp_f32 v66, v66\n\t" \
  "v_exp_f32 v67, v67\n\t" \
  "v_exp_f32 v68, v68\n\t" \
  "v_exp_f32 v69, v69\n\t" \
  "v_exp_f32 v70, v70\n\t" \
  "v_smfmac_f32_32x32x32_bf16 %[o2], v[108:111], v[144:151], %[idx]\n\t" \
  "v_exp_f32 v71, v71\n\t" \
  "ds_read_b128 v[108:111], %[va] offset:512\n\t" \
  "v_exp_f32 v120, v120\n\t" \
  "v_exp_f32 v121, v121\n\t" \
  "v_exp_f32 v122, v122\n\t" \
  "v_exp_f32 v123, v123\n\t" \
  "s_waitcnt lgkmcnt(2)\n\t" \
  "v_mfma_f32_32x32x16_bf16 v[80:95], v[96:99], %[q], 0\n\t" \
  "v_exp_f32 v124, v124\n\t" \
  "v_exp_f32 v125, v125\n\t" \
  "v_exp_f32 v126, v126\n\t" \
  "v_exp_f32 v127, v127\n\t" \
  "v_exp_f32 v72, v72\n\t" \
  "v_mfma_f32_32x32x16_bf16 v[136:151], v[96:99], %[q2], 0\n\t" \
  "v_exp_f32 v73, v73\n\t" \
  "ds_read_b128 v[96:99], %[ka] offset:2048\n\t" \
  "v_exp_f32 v74, v74\n\t" \
  "v_exp_f32 v75, v75\n\t" \
  "v_exp_f32 v76, v76\n\t" \
  "v_exp_f32 v77, v77\n\t" \
  "s_waitcnt lgkmcnt(2)\n\t" \
  "v_smfmac_f32_32x32x32_bf16 %[o], v[104:107], v[64:71], %[idx]\n\t" \
  "v_exp_f32 v78, v78\n\t" \
  "v_exp_f32 v79, v79\n\t" \
  "v_exp_f32 v128, v128\n\t" \
  "v_exp_f32 v129, v129\n\t" \
  "v_exp_f32 v130, v130\n\t" \
  "v_smfmac_f32_32x32x32_bf16 %[o2], v[104:107], v[120:127], %[idx]\n\t" \
  "v_exp_f32 v131, v131\n\t" \
  "ds_read_b128 v[104:107], %[va] offset:1024\n\t" \
  "v_exp_f32 v132, v132\n\t" \
  "v_exp_f32 v133, v133\n\t" \
  "v_exp_f32 v134, v134\n\t" \
  "v_exp_f32 v135, v135\n\t" \
  "v_exp_f32 v80, v80\n\t" \
  "v_exp_f32 v81, v81\n\t" \
  "s_waitcnt lgkmcnt(2)\n\t" \
  "v_smfmac_f32_32x32x32_bf16 %[o], v[108:111], v[72:79], %[idx]\n\t" \
  "v_exp_f32 v82, v82\n\t" \
  "v_exp_f32 v83, v83\n\t" \
  "v_exp_f32 v84, v84\n\t" \
  "v_exp_f32 v85, v85\n\t" \
  "v_exp_f32 v86, v86\n\t" \
  "v_smfmac_f32_32x32x32_bf16 %[o2], v[108:111], v[128:135], %[idx]\n\t" \
  "v_exp_f32 v87, v87\n\t" \
  "ds_read_b128 v[108:111], %[va] offset:1536\n\t" \
  "v_exp_f32 v136, v136\n\t" \
  "v_exp_f32 v137, v137\n\t" \
  "v_exp_f32 v138, v138\n\t" \
  "v_exp_f32 v139, v139\n\t" \
  "s_waitcnt lgkmcnt(2)\n\t" \
  "v_mfma_f32_32x32x16_bf16 v[64:79], v[96:99], %[q], 0\n\t" \
  "v_exp_f32 v140, v140\n\t" \
  "v_exp_f32 v141, v141\n\t" \
  "v_exp_f32 v142, v142\n\t" \
  "v_exp_f32 v143, v143\n\t" \
  "v_exp_f32 v88, v88\n\t" \
  "v_mfma_f32_32x32x16_bf16 v[120:135], v[96:99], %[q2], 0\n\t" \
  "v_exp_f32 v89, v89\n\t" \
  "ds_read_b128 v[96:99], %[ka] offset:3072\n\t" \
  "v_exp_f32 v90, v90\n\t" \
  "v_exp_f32 v91, v91\n\t" \
  "v_exp_f32 v92, v92\n\t" \
  "v_exp_f32 v93, v93\n\t" \
  "s_waitcnt lgkmcnt(2)\n\t" \
  "v_smfmac_f32_32x32x32_bf16 %[o], v[104:107], v[80:87], %[idx]\n\t" \
  "v_exp_f32 v94, v94\n\t" \
  "v_exp_f32 v95, v95\n\t" \
  "v_exp_f32 v144, v144\n\t" \
  "v_exp_f32 v145, v145\n\t" \
  "v_exp_f32 v146, v146\n\t" \
  "v_smfmac_f32_32x32x32_bf16 %[o2], v[104:107], v[136:143], %[idx]\n\t" \
  "v_exp_f32 v147, v147\n\t" \
  "ds_read_b128 v[104:107], %[va] offset:2048\n\t" \
  "v_exp_f32 v148, v148\n\t" \
  "v_exp_f32 v149, v149\n\t" \
  "v_exp_f32 v150, v150\n\t" \
  "v_exp_f32 v151, v151\n\t" \
  "v_exp_f32 v64, v64\n\t" \
  "v_exp_f32 v65, v65\n\t" \
  "s_waitcnt lgkmcnt(2)\n\t" \
  "v_smfmac_f32_32x32x32_bf16 %[o], v[108:111], v[88:95], %[idx]\n\t" \
  "v_exp_f32 v66, v66\n\t" \
  "v_exp_f32 v67, v67\n\t" \
  "v_exp_f32 v68, v68\n\t" \
  "v_exp_f32 v69, v69\n\t" \
  "v_exp_f32 v70, v70\n\t" \
  "v_smfmac_f32_32x32x32_bf16 %[o2], v[108:111], v[144:151], %[idx]\n\t" \
  "v_exp_f32 v71, v71\n\t" \
  "ds_read_b128 v[108:111], %[va] offset:2560\n\t" \
  "v_exp_f32 v120, v120\n\t" \
  "v_exp_f32 v121, v121\n\t" \
  "v_exp_f32 v122, v122\n\t" \
  "v_exp_f32 v123, v123\n\t" \
  "s_waitcnt lgkmcnt(2)\n\t" \
  "v_mfma_f32_32x32x16_bf16 v[80:95], v[96:99], %[q], 0\n\t" \
  "v_exp_f32 v124, v124\n\t" \
  "v_exp_f32 v125, v125\n\t" \
  "v_exp_f32 v126, v126\n\t" \
  "v_exp_f32 v127, v127\n\t" \
  "v_exp_f32 v72, v72\n\t" \
  "v_mfma_f32_32x32x16_bf16 v[136:151], v[96:99], %[q2], 0\n\t" \
  "v_exp_f32 v73, v73\n\t" \
  "ds_read_b128 v[96:99], %[ka] offset:16384\n\t" \
  "v_exp_f32 v74, v74\n\t" \
  "v_exp_f32 v75, v75\n\t" \
  "v_exp_f32 v76, v76\n\t" \
  "v_exp_f32 v77, v77\n\t" \
  "s_waitcnt lgkmcnt(2)\n\t" \
  "v_smfmac_f32_32x32x32_bf16 %[o], v[104:107], v[64:71], %[idx]\n\t" \
  "v_exp_f32 v78, v78\n\t" \
  "v_exp_f32 v79, v79\n\t" \
  "v_exp_f32 v128, v128\n\t" \
  "v_exp_f32 v129, v129\n\t" \
  "v_exp_f32 v130, v130\n\t" \
  "v_smfmac_f32_32x32x32_bf16 %[o2], v[104:107], v[120:127], %[idx]\n\t" \
  "v_exp_f32 v131, v131\n\t" \
  "ds_read_b128 v[104:107], %[va] offset:3072\n\t" \
  "v_exp_f32 v132, v132\n\t" \
  "v_exp_f32 v133, v133\n\t" \
  "v_exp_f32 v134, v134\n\t" \
  "v_exp_f32 v135, v135\n\t" \
  "v_exp_f32 v80, v80\n\t" \
  "v_exp_f32 v81, v81\n\t" \
  "s_waitcnt lgkmcnt(2)\n\t" \
  "v_smfmac_f32_32x32x32_bf16 %[o], v[108:111], v[72:79], %[idx]\n\t" \
  "v_exp_f32 v82, v82\n\t" \
  "v_exp_f32 v83, v83\n\t" \
  "v_exp_f32 v84, v84\n\t" \
  "v_exp_f32 v85, v85\n\t" \
  "v_exp_f32 v86, v86\n\t" \
  "v_smfmac_f32_32x32x32_bf16 %[o2], v[108:111], v[128:135], %[idx]\n\t" \
  "v_exp_f32 v87, v87\n\t" \
  "ds_read_b128 v[108:111], %[va] offset:3584\n\t" \
  "v_exp_f32 v136, v136\n\t" \
  "v_exp_f32 v137, v137\n\t" \
  "v_exp_f32 v138, v138\n\t" \
  "v_exp_f32 v139, v139\n\t" \
  "s_waitcnt lgkmcnt(2)\n\t" \
  "v_mfma_f32_32x32x16_bf16 v[64:79], v[96:99], %[q], 0\n\t" \
  "v_exp_f32 v140, v140\n\t" \
  "v_exp_f32 v141, v141\n\t" \
  "v_exp_f32 v142, v142\n\t" \
  "v_exp_f32 v143, v143\n\t" \
  "v_exp_f32 v88, v88\n\t" \
  "v_mfma_f32_32x32x16_bf16 v[120:135], v[96:99], %[q2], 0\n\t" \
  "v_exp_f32 v89, v89\n\t" \
  "ds_read_b128 v[96:99], %[ka] offset:17408\n\t" \
  "v_exp_f32 v90, v90\n\t" \
  "v_exp_f32 v91, v91\n\t" \
  "v_exp_f32 v92, v92\n\t" \
  "v_exp_f32 v93, v93\n\t" \
  "s_waitcnt lgkmcnt(2)\n\t" \
  "v_smfmac_f32_32x32x32_bf16 %[o], v[104:107], v[80:87], %[idx]\n\t" \
  "v_exp_f32 v94, v94\n\t" \
  "v_exp_f32 v95, v95\n\t" \
  "v_exp_f32 v144, v144\n\t" \
  "v_exp_f32 v145, v145\n\t" \
  "v_exp_f32 v146, v146\n\t" \
  "v_smfmac_f32_32x32x32_bf16 %[o2], v[104:107], v[136:143], %[idx]\n\t" \
  "v_exp_f32 v147, v147\n\t" \
  "v_add_u32 %[va], %[vinc], %[va]\n\t" \
  "ds_read_b128 v[104:107], %[va]\n\t" \
  "v_exp_f32 v148, v148\n\t" \
  "v_exp_f32 v149, v149\n\t" \
  "v_exp_f32 v150, v150\n\t" \
  "v_exp_f32 v151, v151\n\t" \
  "v_add_u32 %[ka], %[kinc], %[ka]\n\t" \
  "s_sub_u32 %[cnt], %[cnt], 1\n\t" \
  "s_cmp_lg_u32 %[cnt], 0\n\t" \
  "s_cbranch_scc1 1b\n\t" \
  "s_waitcnt lgkmcnt(0)\n\t" \
  "s_nop 1\n\t" \
  "v_smfmac_f32_32x32x32_bf16 %[o], v[108:111], v[88:95], %[idx]\n\t" \
  "v_smfmac_f32_32x32x32_bf16 %[o2], v[108:111], v[144:151], %[idx]\n\t" \
  "s_nop 7\n\t" \
  "s_nop 7\n\t"
#define ATTN_LOOP7 \
  "ds_read_b128 v[96:99], %[ka]\n\t" \
  "v_mov_b32 v88, 0\n\t" \
  "v_mov_b32 v89, 0\n\t" \
  "v_mov_b32 v90, 0\n\t" \
  "v_mov_b32 v91, 0\n\t" \
  "v_mov_b32 v92, 0\n\t" \
  "v_mov_b32 v93, 0\n\t" \
  "v_mov_b32 v94, 0\n\t" \
  "v_mov_b32 v95, 0\n\t" \
  "v_mov_b32 v144, 0\n\t" \
  "v_mov_b32 v145, 0\n\t" \
  "v_mov_b32 v146, 0\n\t" \
  "v_mov_b32 v147, 0\n\t" \
  "v_mov_b32 v148, 0\n\t" \
  "v_mov_b32 v149, 0\n\t" \
  "v_mov_b32 v150, 0\n\t" \
  "v_mov_b32 v151, 0\n\t" \
  "s_waitcnt lgkmcnt(0)\n\t" \
  "v_mfma_f32_32x32x16_bf16 v[64:79], v[96:99], %[q], 0\n\t" \
  "v_mfma_f32_32x32x16_bf16 v[120:135], v[96:99], %[q2], 0\n\t" \
  "ds_read_b128 v[108:111], %[va] offset:512\n\t" \
  "ds_read_b128 v[96:99], %[ka] offset:1024\n\t" \
  "ds_read_b128 v[104:107], %[va]\n\t" \
  "s_nop 7\n\t" \
  "s_nop 4\n\t" \
  "v_exp_f32 v64, v64\n\t" \
  "v_exp_f32 v65, v65\n\t" \
  "s_waitcnt lgkmcnt(2)\n\t" \
  "v_smfmac_f32_32x32x32_bf16 %[o], v[108:111], v[88:95], %[idx]\n\t" \
  "v_exp_f32 v66, v66\n\t" \
  "v_exp_f32 v67, v67\n\t" \
  "global_load_dwordx4 v[152:155], %[wofs], %[wo]\n\t" \
  "v_add_u32 %[wofs], 0x10000, %[wofs]\n\t" \
  "v_exp_f32 v68, v68\n\t" \
  "v_exp_f32 v69, v69\n\t" \
  "v_exp_f32 v70, v70\n\t" \
  "v_smfmac_f32_32x32x32_bf16 %[o2], v[108:111], v[144:151], %[idx]\n\t" \
  "v_exp_f32 v71, v71\n\t" \
  "ds_read_b128 v[108:111], %[va] offset:512\n\t" \
  "v_exp_f32 v120, v120\n\t" \
  "v_exp_f32 v121, v121\n\t" \
  "v_exp_f32 v122, v122\n\t" \
  "v_exp_f32 v123, v123\n\t" \
  "s_waitcnt lgkmcnt(2)\n\t" \
  "v_mfma_f32_32x32x16_bf16 v[80:95], v[96:99], %[q], 0\n\t" \
  "v_exp_f32 v124, v124\n\t" \
  "v_exp_f32 v125, v125\n\t" \
  "v_exp_f32 v126, v126\n\t" \
  "v_exp_f32 v127, v127\n\t" \
  "v_exp_f32 v72, v72\n\t" \
  "v_mfma_f32_32x32x16_bf16 v[136:151], v[96:99], %[q2], 0\n\t" \
  "v_exp_f32 v73, v73\n\t" \
  "ds_read_b128 v[96:99], %[ka] offset:2048\n\t" \
  "v_exp_f32 v74, v74\n\t" \
  "v_exp_f32 v75, v75\n\t" \
  "v_exp_f32 v76, v76\n\t" \
  "v_exp_f32 v77, v77\n\t" \
  "s_waitcnt lgkmcnt(2)\n\t" \
  "v_smfmac_f32_32x32x32_bf16 %[o], v[104:107], v[64:71], %[idx]\n\t" \
  "v_exp_f32 v78, v78\n\t" \
  "v_exp_f32 v79, v79\n\t" \
  "v_exp_f32 v128, v128\n\t" \
  "v_exp_f32 v129, v129\n\t" \
  "v_exp_f32 v130, v130\n\t" \
  "v_smfmac_f32_32x32x32_bf16 %[o2], v[104:107], v[120:127], %[idx]\n\t" \
  "v_exp_f32 v131, v131\n\t" \
  "ds_read_b128 v[104:107], %[va] offset:1024\n\t" \
  "v_exp_f32 v132, v132\n\t" \
  "v_exp_f32 v133, v133\n\t" \
  "v_exp_f32 v134, v134\n\t" \
  "v_exp_f32 v135, v135\n\t" \
  "v_exp_f32 v80, v80\n\t" \
  "v_exp_f32 v81, v81\n\t" \
  "s_waitcnt lgkmcnt(2)\n\t" \
  "v_smfmac_f32_32x32x32_bf16 %[o], v[108:111], v[72:79], %[idx]\n\t" \
  "v_exp_f32 v82, v82\n\t" \
  "v_exp_f32 v83, v83\n\t" \
  "v_exp_f32 v84, v84\n\t" \
  "v_exp_f32 v85, v85\n\t" \
  "v_exp_f32 v86, v86\n\t" \
  "v_smfmac_f32_32x32x32_bf16 %[o2], v[108:111], v[128:135], %[idx]\n\t" \
  "v_exp_f32 v87, v87\n\t" \
  "ds_read_b128 v[108:111], %[va] offset:1536\n\t" \
  "v_exp_f32 v136, v136\n\t" \
  "v_exp_f32 v137, v137\n\t" \
  "v_exp_f32 v138, v138\n\t" \
  "v_exp_f32 v139, v139\n\t" \
  "s_waitcnt lgkmcnt(2)\n\t" \
  "v_mfma_f32_32x32x16_bf16 v[64:79], v[96:99], %[q], 0\n\t" \
  "v_exp_f32 v140, v140\n\t" \
  "v_exp_f32 v141, v141\n\t" \
  "v_exp_f32 v142, v142\n\t" \
  "v_exp_f32 v143, v143\n\t" \
  "v_exp_f32 v88, v88\n\t" \
  "v_mfma_f32_32x32x16_bf16 v[120:135], v[96:99], %[q2], 0\n\t" \
  "v_exp_f32 v89, v89\n\t" \
  "ds_read_b128 v[96:99], %[ka] offset:3072\n\t" \
  "v_exp_f32 v90, v90\n\t" \
  "v_exp_f32 v91, v91\n\t" \
  "v_exp_f32 v92, v92\n\t" \
  "v_exp_f32 v93, v93\n\t" \
  "s_waitcnt lgkmcnt(2)\n\t" \
  "v_smfmac_f32_32x32x32_bf16 %[o], v[104:107], v[80:87], %[idx]\n\t" \
  "v_exp_f32 v94, v94\n\t" \
  "v_exp_f32 v95, v95\n\t" \
  "v_exp_f32 v144, v144\n\t" \
  "v_exp_f32 v145, v145\n\t" \
  "v_exp_f32 v146, v146\n\t" \
  "v_smfmac_f32_32x32x32_bf16 %[o2], v[104:107], v[136:143], %[idx]\n\t" \
  "v_exp_f32 v147, v147\n\t" \
  "ds_read_b128 v[104:107], %[va] offset:2048\n\t" \
  "v_exp_f32 v148, v148\n\t" \
  "v_exp_f32 v149, v149\n\t" \
  "v_exp_f32 v150, v150\n\t" \
  "v_exp_f32 v151, v151\n\t" \
  "v_exp_f32 v64, v64\n\t" \
  "v_exp_f32 v65, v65\n\t" \
  "s_waitcnt lgkmcnt(2)\n\t" \
  "v_smfmac_f32_32x32x32_bf16 %[o], v[108:111], v[88:95], %[idx]\n\t" \
  "v_exp_f32 v66, v66\n\t" \
  "v_exp_f32 v67, v67\n\t" \
  "global_load_dwordx4 v[184:187], %[gofs], %[wg]\n\t" \
  "v_exp_f32 v68, v68\n\t" \
  "v_exp_f32 v69, v69\n\t" \
  "v_exp_f32 v70, v70\n\t" \
  "v_smfmac_f32_32x32x32_bf16 %[o2], v[108:111], v[144:151], %[idx]\n\t" \
  "v_exp_f32 v71, v71\n\t" \
  "ds_read_b128 v[108:111], %[va] offset:2560\n\t" \
  "v_exp_f32 v120, v120\n\t" \
  "v_exp_f32 v121, v121\n\t" \
  "v_exp_f32 v122, v122\n\t" \
  "v_exp_f32 v123, v123\n\t" \
  "s_waitcnt lgkmcnt(2)\n\t" \
  "v_mfma_f32_32x32x16_bf16 v[80:95], v[96:99], %[q], 0\n\t" \
  "v_exp_f32 v124, v124\n\t" \
  "v_exp_f32 v125, v125\n\t" \
  "v_exp_f32 v126, v126\n\t" \
  "v_exp_f32 v127, v127\n\t" \
  "v_exp_f32 v72, v72\n\t" \
  "v_mfma_f32_32x32x16_bf16 v[136:151], v[96:99], %[q2], 0\n\t" \
  "v_exp_f32 v73, v73\n\t" \
  "ds_read_b128 v[96:99], %[ka] offset:16384\n\t" \
  "v_exp_f32 v74, v74\n\t" \
  "v_exp_f32 v75, v75\n\t" \
  "v_exp_f32 v76, v76\n\t" \
  "v_exp_f32 v77, v77\n\t" \
  "s_waitcnt lgkmcnt(2)\n\t" \
  "v_smfmac_f32_32x32x32_bf16 %[o], v[104:107], v[64:71], %[idx]\n\t" \
  "v_exp_f32 v78, v78\n\t" \
  "v_exp_f32 v79, v79\n\t" \
  "v_exp_f32 v128, v128\n\t" \
  "v_exp_f32 v129, v129\n\t" \
  "v_exp_f32 v130, v130\n\t" \
  "v_smfmac_f32_32x32x32_bf16 %[o2], v[104:107], v[120:127], %[idx]\n\t" \
  "v_exp_f32 v131, v131\n\t" \
  "ds_read_b128 v[104:107], %[va] offset:3072\n\t" \
  "v_exp_f32 v132, v132\n\t" \
  "v_exp_f32 v133, v133\n\t" \
  "v_exp_f32 v134, v134\n\t" \
  "v_exp_f32 v135, v135\n\t" \
  "v_exp_f32 v80, v80\n\t" \
  "v_exp_f32 v81, v81\n\t" \
  "s_waitcnt lgkmcnt(2)\n\t" \
  "v_smfmac_f32_32x32x32_bf16 %[o], v[108:111], v[72:79], %[idx]\n\t" \
  "v_exp_f32 v82, v82\n\t" \
  "v_exp_f32 v83, v83\n\t" \
  "v_exp_f32 v84, v84\n\t" \
  "v_exp_f32 v85, v85\n\t" \
  "v_exp_f32 v86, v86\n\t" \
  "v_smfmac_f32_32x32x32_bf16 %[o2], v[108:111], v[128:135], %[idx]\n\t" \
  "v_exp_f32 v87, v87\n\t" \
  "ds_read_b128 v[108:111], %[va] offset:3584\n\t" \
  "v_exp_f32 v136, v136\n\t" \
  "v_exp_f32 v137, v137\n\t" \
  "v_exp_f32 v138, v138\n\t" \
  "v_exp_f32 v139, v139\n\t" \
  "s_waitcnt lgkmcnt(2)\n\t" \
  "v_mfma_f32_32x32x16_bf16 v[64:79], v[96:99], %[q], 0\n\t" \
  "v_exp_f32 v140, v140\n\t" \
  "v_exp_f32 v141, v141\n\t" \
  "v_exp_f32 v142, v142\n\t" \
  "v_exp_f32 v143, v143\n\t" \
  "v_exp_f32 v88, v88\n\t" \
  "v_mfma_f32_32x32x16_bf16 v[120:135], v[96:99], %[q2], 0\n\t" \
  "v_exp_f32 v89, v89\n\t" \
  "ds_read_b128 v[96:99], %[ka] offset:17408\n\t" \
  "v_exp_f32 v90, v90\n\t" \
  "v_exp_f32 v91, v91\n\t" \
  "v_exp_f32 v92, v92\n\t" \
  "v_exp_f32 v93, v93\n\t" \
  "s_waitcnt lgkmcnt(2)\n\t" \
  "v_smfmac_f32_32x32x32_bf16 %[o], v[104:107], v[80:87], %[idx]\n\t" \
  "v_exp_f32 v94, v94\n\t" \
  "v_exp_f32 v95, v95\n\t" \
  "v_exp_f32 v144, v144\n\t" \
  "v_exp_f32 v145, v145\n\t" \
  "v_exp_f32 v146, v146\n\t" \
  "v_smfmac_f32_32x32x32_bf16 %[o2], v[104:107], v[136:143], %[idx]\n\t" \
  "v_exp_f32 v147, v147\n\t" \
  "v_add_u32 %[va], %[vinc], %[va]\n\t" \
  "ds_read_b128 v[104:107], %[va]\n\t" \
  "v_exp_f32 v148, v148\n\t" \
  "v_exp_f32 v149, v149\n\t" \
  "v_exp_f32 v150, v150\n\t" \
  "v_exp_f32 v151, v151\n\t" \
  "v_add_u32 %[ka], %[kinc], %[ka]\n\t" \
  "v_exp_f32 v64, v64\n\t" \
  "v_exp_f32 v65, v65\n\t" \
  "s_waitcnt lgkmcnt(2)\n\t" \
  "v_smfmac_f32_32x32x32_bf16 %[o], v[108:111], v[88:95], %[idx]\n\t" \
  "v_exp_f32 v66, v66\n\t" \
  "v_exp_f32 v67, v67\n\t" \
  "global_load_dwordx4 v[156:159], %[wofs], %[wo]\n\t" \
  "v_add_u32 %[wofs], 0x10000, %[wofs]\n\t" \
  "v_exp_f32 v68, v68\n\t" \
  "v_exp_f32 v69, v69\n\t" \
  "v_exp_f32 v70, v70\n\t" \
  "v_smfmac_f32_32x32x32_bf16 %[o2], v[108:111], v[144:151], %[idx]\n\t" \
  "v_exp_f32 v71, v71\n\t" \
  "ds_read_b128 v[108:111], %[va] offset:512\n\t" \
  "v_exp_f32 v120, v120\n\t" \
  "v_exp_f32 v121, v121\n\t" \
  "v_exp_f32 v122, v122\n\t" \
  "v_exp_f32 v123, v123\n\t" \
  "s_waitcnt lgkmcnt(2)\n\t" \
  "v_mfma_f32_32x32x16_bf16 v[80:95], v[96:99], %[q], 0\n\t" \
  "v_exp_f32 v124, v124\n\t" \
  "v_exp_f32 v125, v125\n\t" \
  "v_exp_f32 v126, v126\n\t" \
  "v_exp_f32 v127, v127\n\t" \
  "v_exp_f32 v72, v72\n\t" \
  "v_mfma_f32_32x32x16_bf16 v[136:151], v[96:99], %[q2], 0\n\t" \
  "v_exp_f32 v73, v73\n\t" \
  "ds_read_b128 v[96:99], %[ka] offset:2048\n\t" \
  "v_exp_f32 v74, v74\n\t" \
  "v_exp_f32 v75, v75\n\t" \
  "v_exp_f32 v76, v76\n\t" \
  "v_exp_f32 v77, v77\n\t" \
  "s_waitcnt lgkmcnt(2)\n\t" \
  "v_smfmac_f32_32x32x32_bf16 %[o], v[104:107], v[64:71], %[idx]\n\t" \
  "v_exp_f32 v78, v78\n\t" \
  "v_exp_f32 v79, v79\n\t" \
  "v_exp_f32 v128, v128\n\t" \
  "v_exp_f32 v129, v129\n\t" \
  "v_exp_f32 v130, v130\n\t" \
  "v_smfmac_f32_32x32x32_bf16 %[o2], v[104:107], v[120:127], %[idx]\n\t" \
  "v_exp_f32 v131, v131\n\t" \
  "ds_read_b128 v[104:107], %[va] offset:1024\n\t" \
  "v_exp_f32 v132, v132\n\t" \
  "v_exp_f32 v133, v133\n\t" \
  "v_exp_f32 v134, v134\n\t" \
  "v_exp_f32 v135, v135\n\t" \
  "v_exp_f32 v80, v80\n\t" \
  "v_exp_f32 v81, v81\n\t" \
  "s_waitcnt lgkmcnt(2)\n\t" \
  "v_smfmac_f32_32x32x32_bf16 %[o], v[108:111], v[72:79], %[idx]\n\t" \
  "v_exp_f32 v82, v82\n\t" \
  "v_exp_f32 v83, v83\n\t" \
  "s_waitcnt vmcnt(3)\n\t" \
  "s_barrier\n\t" \
  "v_exp_f32 v84, v84\n\t" \
  "v_exp_f32 v85, v85\n\t" \
  "v_exp_f32 v86, v86\n\t" \
  "v_smfmac_f32_32x32x32_bf16 %[o2], v[108:111], v[128:135], %[idx]\n\t" \
  "v_exp_f32 v87, v87\n\t" \
  "ds_read_b128 v[108:111], %[va] offset:1536\n\t" \
  "v_exp_f32 v136, v136\n\t" \
  "v_exp_f32 v137, v137\n\t" \
  "v_exp_f32 v138, v138\n\t" \
  "v_exp_f32 v139, v139\n\t" \
  "s_waitcnt lgkmcnt(2)\n\t" \
  "v_mfma_f32_32x32x16_bf16 v[64:79], v[96:99], %[q], 0\n\t" \
  "v_exp_f32 v140, v140\n\t" \
  "v_exp_f32 v141, v141\n\t" \
  "v_exp_f32 v142, v142\n\t" \
  "v_exp_f32 v143, v143\n\t" \
  "v_exp_f32 v88, v88\n\t" \
  "v_mfma_f32_32x32x16_bf16 v[120:135], v[96:99], %[q2], 0\n\t" \
  "v_exp_f32 v89, v89\n\t" \
  "ds_read_b128 v[96:99], %[ka] offset:3072\n\t" \
  "v_exp_f32 v90, v90\n\t" \
  "v_exp_f32 v91, v91\n\t" \
  "v_exp_f32 v92, v92\n\t" \
  "v_exp_f32 v93, v93\n\t" \
  "s_waitcnt lgkmcnt(2)\n\t" \
  "v_smfmac_f32_32x32x32_bf16 %[o], v[104:107], v[80:87], %[idx]\n\t" \
  "v_exp_f32 v94, v94\n\t" \
  "v_exp_f32 v95, v95\n\t" \
  "v_exp_f32 v144, v144\n\t" \
  "v_exp_f32 v145, v145\n\t" \
  "v_exp_f32 v146, v146\n\t" \
  "v_smfmac_f32_32x32x32_bf16 %[o2], v[104:107], v[136:143], %[idx]\n\t" \
  "v_exp_f32 v147, v147\n\t" \
  "ds_read_b128 v[104:107], %[va] offset:2048\n\t" \
  "v_exp_f32 v148, v148\n\t" \
  "v_exp_f32 v149, v149\n\t" \
  "v_exp_f32 v150, v150\n\t" \
  "v_exp_f32 v151, v151\n\t" \
  "v_exp_f32 v64, v64\n\t" \
  "v_exp_f32 v65, v65\n\t" \
  "s_waitcnt lgkmcnt(2)\n\t" \
  "v_smfmac_f32_32x32x32_bf16 %[o], v[108:111], v[88:95], %[idx]\n\t" \
  "v_exp_f32 v66, v66\n\t" \
  "v_exp_f32 v67, v67\n\t" \
  "global_load_dwordx4 v[188:191], %[gofs], %[wu]\n\t" \
  "v_exp_f32 v68, v68\n\t" \
  "v_exp_f32 v69, v69\n\t" \
  "v_exp_f32 v70, v70\n\t" \
  "v_smfmac_f32_32x32x32_bf16 %[o2], v[108:111], v[144:151], %[idx]\n\t" \
  "v_exp_f32 v71, v71\n\t" \
  "ds_read_b128 v[108:111], %[va] offset:2560\n\t" \
  "v_exp_f32 v120, v120\n\t" \
  "v_exp_f32 v121, v121\n\t" \
  "v_exp_f32 v122, v122\n\t" \
  "v_exp_f32 v123, v123\n\t" \
  "s_waitcnt lgkmcnt(2)\n\t" \
  "v_mfma_f32_32x32x16_bf16 v[80:95], v[96:99], %[q], 0\n\t" \
  "v_exp_f32 v124, v124\n\t" \
  "v_exp_f32 v125, v125\n\t" \
  "v_exp_f32 v126, v126\n\t" \
  "v_exp_f32 v127, v127\n\t" \
  "v_exp_f32 v72, v72\n\t" \
  "v_mfma_f32_32x32x16_bf16 v[136:151], v[96:99], %[q2], 0\n\t" \
  "v_exp_f32 v73, v73\n\t" \
  "ds_read_b128 v[96:99], %[ka] offset:16384\n\t" \
  "v_exp_f32 v74, v74\n\t" \
  "v_exp_f32 v75, v75\n\t" \
  "v_exp_f32 v76, v76\n\t" \
  "v_exp_f32 v77, v77\n\t" \
  "s_waitcnt lgkmcnt(2)\n\t" \
  "v_smfmac_f32_32x32x32_bf16 %[o], v[104:107], v[64:71], %[idx]\n\t" \
  "v_exp_f32 v78, v78\n\t" \
  "v_exp_f32 v79, v79\n\t" \
  "v_exp_f32 v128, v128\n\t" \
  "v_exp_f32 v129, v129\n\t" \
  "v_exp_f32 v130, v130\n\t" \
  "v_smfmac_f32_32x32x32_bf16 %[o2], v[104:107], v[120:127], %[idx]\n\t" \
  "v_exp_f32 v131, v131\n\t" \
  "ds_read_b128 v[104:107], %[va] offset:3072\n\t" \
  "v_exp_f32 v132, v132\n\t" \
  "v_exp_f32 v133, v133\n\t" \
  "v_exp_f32 v134, v134\n\t" \
  "v_exp_f32 v135, v135\n\t" \
  "v_exp_f32 v80, v80\n\t" \
  "v_exp_f32 v81, v81\n\t" \
  "s_waitcnt lgkmcnt(2)\n\t" \
  "v_smfmac_f32_32x32x32_bf16 %[o], v[108:111], v[72:79], %[idx]\n\t" \
  "v_exp_f32 v82, v82\n\t" \
  "v_exp_f32 v83, v83\n\t" \
  "v_exp_f32 v84, v84\n\t" \
  "v_exp_f32 v85, v85\n\t" \
  "v_exp_f32 v86, v86\n\t" \
  "v_smfmac_f32_32x32x32_bf16 %[o2], v[108:111], v[128:135], %[idx]\n\t" \
  "v_exp_f32 v87, v87\n\t" \
  "ds_read_b128 v[108:111], %[va] offset:3584\n\t" \
  "v_exp_f32 v136, v136\n\t" \
  "v_exp_f32 v137, v137\n\t" \
  "v_exp_f32 v138, v138\n\t" \
  "v_exp_f32 v139, v139\n\t" \
  "s_waitcnt lgkmcnt(2)\n\t" \
  "v_mfma_f32_32x32x16_bf16 v[64:79], v[96:99], %[q], 0\n\t" \
  "v_exp_f32 v140, v140\n\t" \
  "v_exp_f32 v141, v141\n\t" \
  "v_exp_f32 v142, v142\n\t" \
  "v_exp_f32 v143, v143\n\t" \
  "v_exp_f32 v88, v88\n\t" \
  "v_mfma_f32_32x32x16_bf16 v[120:135], v[96:99], %[q2], 0\n\t" \
  "v_exp_f32 v89, v89\n\t" \
  "ds_read_b128 v[96:99], %[ka] offset:17408\n\t" \
  "v_exp_f32 v90, v90\n\t" \
  "v_exp_f32 v91, v91\n\t" \
  "v_exp_f32 v92, v92\n\t" \
  "v_exp_f32 v93, v93\n\t" \
  "s_waitcnt lgkmcnt(2)\n\t" \
  "v_smfmac_f32_32x32x32_bf16 %[o], v[104:107], v[80:87], %[idx]\n\t" \
  "v_exp_f32 v94, v94\n\t" \
  "v_exp_f32 v95, v95\n\t" \
  "v_exp_f32 v144, v144\n\t" \
  "v_exp_f32 v145, v145\n\t" \
  "v_exp_f32 v146, v146\n\t" \
  "v_smfmac_f32_32x32x32_bf16 %[o2], v[104:107], v[136:143], %[idx]\n\t" \
  "v_exp_f32 v147, v147\n\t" \
  "v_add_u32 %[va], %[vinc], %[va]\n\t" \
  "ds_read_b128 v[104:107], %[va]\n\t" \
  "v_exp_f32 v148, v148\n\t" \
  "v_exp_f32 v149, v149\n\t" \
  "v_exp_f32 v150, v150\n\t" \
  "v_exp_f32 v151, v151\n\t" \
  "v_add_u32 %[ka], %[kinc], %[ka]\n\t" \
  "v_exp_f32 v64, v64\n\t" \
  "v_exp_f32 v65, v65\n\t" \
  "s_waitcnt lgkmcnt(2)\n\t" \
  "v_smfmac_f32_32x32x32_bf16 %[o], v[108:111], v[88:95], %[idx]\n\t" \
  "v_exp_f32 v66, v66\n\t" \
  "v_exp_f32 v67, v67\n\t" \
  "global_load_dwordx4 v[160:163], %[wofs], %[wo]\n\t" \
  "v_add_u32 %[wofs], 0x10000, %[wofs]\n\t" \
  "v_exp_f32 v68, v68\n\t" \
  "v_exp_f32 v69, v69\n\t" \
  "v_exp_f32 v70, v70\n\t" \
  "v_smfmac_f32_32x32x32_bf16 %[o2], v[108:111], v[144:151], %[idx]\n\t" \
  "v_exp_f32 v71, v71\n\t" \
  "ds_read_b128 v[108:111], %[va] offset:512\n\t" \
  "v_exp_f32 v120, v120\n\t" \
  "v_exp_f32 v121, v121\n\t" \
  "v_exp_f32 v122, v122\n\t" \
  "v_exp_f32 v123, v123\n\t" \
  "s_waitcnt lgkmcnt(2)\n\t" \
  "v_mfma_f32_32x32x16_bf16 v[80:95], v[96:99], %[q], 0\n\t" \
  "v_exp_f32 v124, v124\n\t" \
  "v_exp_f32 v125, v125\n\t" \
  "v_exp_f32 v126, v126\n\t" \
  "v_exp_f32 v127, v127\n\t" \
  "v_exp_f32 v72, v72\n\t" \
  "v_mfma_f32_32x32x16_bf16 v[136:151], v[96:99], %[q2], 0\n\t" \
  "v_exp_f32 v73, v73\n\t" \
  "ds_read_b128 v[96:99], %[ka] offset:2048\n\t" \
  "v_exp_f32 v74, v74\n\t" \
  "v_exp_f32 v75, v75\n\t" \
  "v_exp_f32 v76, v76\n\t" \
  "v_exp_f32 v77, v77\n\t" \
  "s_waitcnt lgkmcnt(2)\n\t" \
  "v_smfmac_f32_32x32x32_bf16 %[o], v[104:107], v[64:71], %[idx]\n\t" \
  "v_exp_f32 v78, v78\n\t" \
  "v_exp_f32 v79, v79\n\t" \
  "v_exp_f32 v128, v128\n\t" \
  "v_exp_f32 v129, v129\n\t" \
  "v_exp_f32 v130, v130\n\t" \
  "v_smfmac_f32_32x32x32_bf16 %[o2], v[104:107], v[120:127], %[idx]\n\t" \
  "v_exp_f32 v131, v131\n\t" \
  "ds_read_b128 v[104:107], %[va] offset:1024\n\t" \
  "v_exp_f32 v132, v132\n\t" \
  "v_exp_f32 v133, v133\n\t" \
  "v_exp_f32 v134, v134\n\t" \
  "v_exp_f32 v135, v135\n\t" \
  "v_exp_f32 v80, v80\n\t" \
  "v_exp_f32 v81, v81\n\t" \
  "s_waitcnt lgkmcnt(2)\n\t" \
  "v_smfmac_f32_32x32x32_bf16 %[o], v[108:111], v[72:79], %[idx]\n\t" \
  "v_exp_f32 v82, v82\n\t" \
  "v_exp_f32 v83, v83\n\t" \
  "v_exp_f32 v84, v84\n\t" \
  "v_exp_f32 v85, v85\n\t" \
  "v_exp_f32 v86, v86\n\t" \
  "v_smfmac_f32_32x32x32_bf16 %[o2], v[108:111], v[128:135], %[idx]\n\t" \
  "v_exp_f32 v87, v87\n\t" \
  "ds_read_b128 v[108:111], %[va] offset:1536\n\t" \
  "v_exp_f32 v136, v136\n\t" \
  "v_exp_f32 v137, v137\n\t" \
  "v_exp_f32 v138, v138\n\t" \
  "v_exp_f32 v139, v139\n\t" \
  "s_waitcnt lgkmcnt(2)\n\t" \
  "v_mfma_f32_32x32x16_bf16 v[64:79], v[96:99], %[q], 0\n\t" \
  "v_exp_f32 v140, v140\n\t" \
  "v_exp_f32 v141, v141\n\t" \
  "v_exp_f32 v142, v142\n\t" \
  "v_exp_f32 v143, v143\n\t" \
  "v_exp_f32 v88, v88\n\t" \
  "v_mfma_f32_32x32x16_bf16 v[120:135], v[96:99], %[q2], 0\n\t" \
  "v_exp_f32 v89, v89\n\t" \
  "ds_read_b128 v[96:99], %[ka] offset:3072\n\t" \
  "v_exp_f32 v90, v90\n\t" \
  "v_exp_f32 v91, v91\n\t" \
  "v_exp_f32 v92, v92\n\t" \
  "v_exp_f32 v93, v93\n\t" \
  "s_waitcnt lgkmcnt(2)\n\t" \
  "v_smfmac_f32_32x32x32_bf16 %[o], v[104:107], v[80:87], %[idx]\n\t" \
  "v_exp_f32 v94, v94\n\t" \
  "v_exp_f32 v95, v95\n\t" \
  "v_exp_f32 v144, v144\n\t" \
  "v_exp_f32 v145, v145\n\t" \
  "v_exp_f32 v146, v146\n\t" \
  "v_smfmac_f32_32x32x32_bf16 %[o2], v[104:107], v[136:143], %[idx]\n\t" \
  "v_exp_f32 v147, v147\n\t" \
  "ds_read_b128 v[104:107], %[va] offset:2048\n\t" \
  "v_exp_f32 v148, v148\n\t" \
  "v_exp_f32 v149, v149\n\t" \
  "v_exp_f32 v150, v150\n\t" \
  "v_exp_f32 v151, v151\n\t" \
  "v_exp_f32 v64, v64\n\t" \
  "v_exp_f32 v65, v65\n\t" \
  "s_waitcnt lgkmcnt(2)\n\t" \
  "v_smfmac_f32_32x32x32_bf16 %[o], v[108:111], v[88:95], %[idx]\n\t" \
  "v_exp_f32 v66, v66\n\t" \
  "v_exp_f32 v67, v67\n\t" \
  "global_load_dword v192, %[dofs], %[wd]\n\t" \
  "v_add_u32 %[dofs], 0x80000, %[dofs]\n\t" \
  "v_exp_f32 v68, v68\n\t" \
  "v_exp_f32 v69, v69\n\t" \
  "v_exp_f32 v70, v70\n\t" \
  "v_smfmac_f32_32x32x32_bf16 %[o2], v[108:111], v[144:151], %[idx]\n\t" \
  "v_exp_f32 v71, v71\n\t" \
  "ds_read_b128 v[108:111], %[va] offset:2560\n\t" \
  "v_exp_f32 v120, v120\n\t" \
  "v_exp_f32 v121, v121\n\t" \
  "v_exp_f32 v122, v122\n\t" \
  "v_exp_f32 v123, v123\n\t" \
  "s_waitcnt lgkmcnt(2)\n\t" \
  "v_mfma_f32_32x32x16_bf16 v[80:95], v[96:99], %[q], 0\n\t" \
  "v_exp_f32 v124, v124\n\t" \
  "v_exp_f32 v125, v125\n\t" \
  "v_exp_f32 v126, v126\n\t" \
  "v_exp_f32 v127, v127\n\t" \
  "v_exp_f32 v72, v72\n\t" \
  "v_mfma_f32_32x32x16_bf16 v[136:151], v[96:99], %[q2], 0\n\t" \
  "v_exp_f32 v73, v73\n\t" \
  "ds_read_b128 v[96:99], %[ka] offset:16384\n\t" \
  "v_exp_f32 v74, v74\n\t" \
  "v_exp_f32 v75, v75\n\t" \
  "v_exp_f32 v76, v76\n\t" \
  "v_exp_f32 v77, v77\n\t" \
  "s_waitcnt lgkmcnt(2)\n\t" \
  "v_smfmac_f32_32x32x32_bf16 %[o], v[104:107], v[64:71], %[idx]\n\t" \
  "v_exp_f32 v78, v78\n\t" \
  "v_exp_f32 v79, v79\n\t" \
  "v_exp_f32 v128, v128\n\t" \
  "v_exp_f32 v129, v129\n\t" \
  "v_exp_f32 v130, v130\n\t" \
  "v_smfmac_f32_32x32x32_bf16 %[o2], v[104:107], v[120:127], %[idx]\n\t" \
  "v_exp_f32 v131, v131\n\t" \
  "ds_read_b128 v[104:107], %[va] offset:3072\n\t" \
  "v_exp_f32 v132, v132\n\t" \
  "v_exp_f32 v133, v133\n\t" \
  "v_exp_f32 v134, v134\n\t" \
  "v_exp_f32 v135, v135\n\t" \
  "v_exp_f32 v80, v80\n\t" \
  "v_exp_f32 v81, v81\n\t" \
  "s_waitcnt lgkmcnt(2)\n\t" \
  "v_smfmac_f32_32x32x32_bf16 %[o], v[108:111], v[72:79], %[idx]\n\t" \
  "v_exp_f32 v82, v82\n\t" \
  "v_exp_f32 v83, v83\n\t" \
  "v_exp_f32 v84, v84\n\t" \
  "v_exp_f32 v85, v85\n\t" \
  "v_exp_f32 v86, v86\n\t" \
  "v_smfmac_f32_32x32x32_bf16 %[o2], v[108:111], v[128:135], %[idx]\n\t" \
  "v_exp_f32 v87, v87\n\t" \
  "ds_read_b128 v[108:111], %[va] offset:3584\n\t" \
  "v_exp_f32 v136, v136\n\t" \
  "v_exp_f32 v137, v137\n\t" \
  "v_exp_f32 v138, v138\n\t" \
  "v_exp_f32 v139, v139\n\t" \
  "s_waitcnt lgkmcnt(2)\n\t" \
  "v_mfma_f32_32x32x16_bf16 v[64:79], v[96:99], %[q], 0\n\t" \
  "v_exp_f32 v140, v140\n\t" \
  "v_exp_f32 v141, v141\n\t" \
  "v_exp_f32 v142, v142\n\t" \
  "v_exp_f32 v143, v143\n\t" \
  "v_exp_f32 v88, v88\n\t" \
  "v_mfma_f32_32x32x16_bf16 v[120:135], v[96:99], %[q2], 0\n\t" \
  "v_exp_f32 v89, v89\n\t" \
  "ds_read_b128 v[96:99], %[ka] offset:17408\n\t" \
  "v_exp_f32 v90, v90\n\t" \
  "v_exp_f32 v91, v91\n\t" \
  "v_exp_f32 v92, v92\n\t" \
  "v_exp_f32 v93, v93\n\t" \
  "s_waitcnt lgkmcnt(2)\n\t" \
  "v_smfmac_f32_32x32x32_bf16 %[o], v[104:107], v[80:87], %[idx]\n\t" \
  "v_exp_f32 v94, v94\n\t" \
  "v_exp_f32 v95, v95\n\t" \
  "v_exp_f32 v144, v144\n\t" \
  "v_exp_f32 v145, v145\n\t" \
  "v_exp_f32 v146, v146\n\t" \
  "v_smfmac_f32_32x32x32_bf16 %[o2], v[104:107], v[136:143], %[idx]\n\t" \
  "v_exp_f32 v147, v147\n\t" \
  "v_add_u32 %[va], %[vinc], %[va]\n\t" \
  "ds_read_b128 v[104:107], %[va]\n\t" \
  "v_exp_f32 v148, v148\n\t" \
  "v_exp_f32 v149, v149\n\t" \
  "v_exp_f32 v150, v150\n\t" \
  "v_exp_f32 v151, v151\n\t" \
  "v_add_u32 %[ka], %[kinc], %[ka]\n\t" \
  "v_exp_f32 v64, v64\n\t" \
  "v_exp_f32 v65, v65\n\t" \
  "s_waitcnt lgkmcnt(2)\n\t" \
  "v_smfmac_f32_32x32x32_bf16 %[o], v[108:111], v[88:95], %[idx]\n\t" \
  "v_exp_f32 v66, v66\n\t" \
  "v_exp_f32 v67, v67\n\t" \
  "global_load_dwordx4 v[164:167], %[wofs], %[wo]\n\t" \
  "v_add_u32 %[wofs], 0x10000, %[wofs]\n\t" \
  "v_exp_f32 v68, v68\n\t" \
  "v_exp_f32 v69, v69\n\t" \
  "v_exp_f32 v70, v70\n\t" \
  "v_smfmac_f32_32x32x32_bf16 %[o2], v[108:111], v[144:151], %[idx]\n\t" \
  "v_exp_f32 v71, v71\n\t" \
  "ds_read_b128 v[108:111], %[va] offset:512\n\t" \
  "v_exp_f32 v120, v120\n\t" \
  "v_exp_f32 v121, v121\n\t" \
  "v_exp_f32 v122, v122\n\t" \
  "v_exp_f32 v123, v123\n\t" \
  "s_waitcnt lgkmcnt(2)\n\t" \
  "v_mfma_f32_32x32x16_bf16 v[80:95], v[96:99], %[q], 0\n\t" \
  "v_exp_f32 v124, v124\n\t" \
  "v_exp_f32 v125, v125\n\t" \
  "v_exp_f32 v126, v126\n\t" \
  "v_exp_f32 v127, v127\n\t" \
  "v_exp_f32 v72, v72\n\t" \
  "v_mfma_f32_32x32x16_bf16 v[136:151], v[96:99], %[q2], 0\n\t" \
  "v_exp_f32 v73, v73\n\t" \
  "ds_read_b128 v[96:99], %[ka] offset:2048\n\t" \
  "v_exp_f32 v74, v74\n\t" \
  "v_exp_f32 v75, v75\n\t" \
  "v_exp_f32 v76, v76\n\t" \
  "v_exp_f32 v77, v77\n\t" \
  "s_waitcnt lgkmcnt(2)\n\t" \
  "v_smfmac_f32_32x32x32_bf16 %[o], v[104:107], v[64:71], %[idx]\n\t" \
  "v_exp_f32 v78, v78\n\t" \
  "v_exp_f32 v79, v79\n\t" \
  "v_exp_f32 v128, v128\n\t" \
  "v_exp_f32 v129, v129\n\t" \
  "v_exp_f32 v130, v130\n\t" \
  "v_smfmac_f32_32x32x32_bf16 %[o2], v[104:107], v[120:127], %[idx]\n\t" \
  "v_exp_f32 v131, v131\n\t" \
  "ds_read_b128 v[104:107], %[va] offset:1024\n\t" \
  "v_exp_f32 v132, v132\n\t" \
  "v_exp_f32 v133, v133\n\t" \
  "v_exp_f32 v134, v134\n\t" \
  "v_exp_f32 v135, v135\n\t" \
  "v_exp_f32 v80, v80\n\t" \
  "v_exp_f32 v81, v81\n\t" \
  "s_waitcnt lgkmcnt(2)\n\t" \
  "v_smfmac_f32_32x32x32_bf16 %[o], v[108:111], v[72:79], %[idx]\n\t" \
  "v_exp_f32 v82, v82\n\t" \
  "v_exp_f32 v83, v83\n\t" \
  "v_exp_f32 v84, v84\n\t" \
  "v_exp_f32 v85, v85\n\t" \
  "v_exp_f32 v86, v86\n\t" \
  "v_smfmac_f32_32x32x32_bf16 %[o2], v[108:111], v[128:135], %[idx]\n\t" \
  "v_exp_f32 v87, v87\n\t" \
  "ds_read_b128 v[108:111], %[va] offset:1536\n\t" \
  "v_exp_f32 v136, v136\n\t" \
  "v_exp_f32 v137, v137\n\t" \
  "v_exp_f32 v138, v138\n\t" \
  "v_exp_f32 v139, v139\n\t" \
  "s_waitcnt lgkmcnt(2)\n\t" \
  "v_mfma_f32_32x32x16_bf16 v[64:79], v[96:99], %[q], 0\n\t" \
  "v_exp_f32 v140, v140\n\t" \
  "v_exp_f32 v141, v141\n\t" \
  "v_exp_f32 v142, v142\n\t" \
  "v_exp_f32 v143, v143\n\t" \
  "v_exp_f32 v88, v88\n\t" \
  "v_mfma_f32_32x32x16_bf16 v[120:135], v[96:99], %[q2], 0\n\t" \
  "v_exp_f32 v89, v89\n\t" \
  "ds_read_b128 v[96:99], %[ka] offset:3072\n\t" \
  "v_exp_f32 v90, v90\n\t" \
  "v_exp_f32 v91, v91\n\t" \
  "v_exp_f32 v92, v92\n\t" \
  "v_exp_f32 v93, v93\n\t" \
  "s_waitcnt lgkmcnt(2)\n\t" \
  "v_smfmac_f32_32x32x32_bf16 %[o], v[104:107], v[80:87], %[idx]\n\t" \
  "v_exp_f32 v94, v94\n\t" \
  "v_exp_f32 v95, v95\n\t" \
  "v_exp_f32 v144, v144\n\t" \
  "v_exp_f32 v145, v145\n\t" \
  "v_exp_f32 v146, v146\n\t" \
  "v_smfmac_f32_32x32x32_bf16 %[o2], v[104:107], v[136:143], %[idx]\n\t" \
  "v_exp_f32 v147, v147\n\t" \
  "ds_read_b128 v[104:107], %[va] offset:2048\n\t" \
  "v_exp_f32 v148, v148\n\t" \
  "v_exp_f32 v149, v149\n\t" \
  "v_exp_f32 v150, v150\n\t" \
  "v_exp_f32 v151, v151\n\t" \
  "v_exp_f32 v64, v64\n\t" \
  "v_exp_f32 v65, v65\n\t" \
  "s_waitcnt lgkmcnt(2)\n\t" \
  "v_smfmac_f32_32x32x32_bf16 %[o], v[108:111], v[88:95], %[idx]\n\t" \
  "v_exp_f32 v66, v66\n\t" \
  "v_exp_f32 v67, v67\n\t" \
  "global_load_dword v193, %[dofs], %[wd]\n\t" \
  "v_add_u32 %[dofs], 0x80000, %[dofs]\n\t" \
  "v_exp_f32 v68, v68\n\t" \
  "v_exp_f32 v69, v69\n\t" \
  "v_exp_f32 v70, v70\n\t" \
  "v_smfmac_f32_32x32x32_bf16 %[o2], v[108:111], v[144:151], %[idx]\n\t" \
  "v_exp_f32 v71, v71\n\t" \
  "ds_read_b128 v[108:111], %[va] offset:2560\n\t" \
  "v_exp_f32 v120, v120\n\t" \
  "v_exp_f32 v121, v121\n\t" \
  "v_exp_f32 v122, v122\n\t" \
  "v_exp_f32 v123, v123\n\t" \
  "s_waitcnt lgkmcnt(2)\n\t" \
  "v_mfma_f32_32x32x16_bf16 v[80:95], v[96:99], %[q], 0\n\t" \
  "v_exp_f32 v124, v124\n\t" \
  "v_exp_f32 v125, v125\n\t" \
  "v_exp_f32 v126, v126\n\t" \
  "v_exp_f32 v127, v127\n\t" \
  "v_exp_f32 v72, v72\n\t" \
  "v_mfma_f32_32x32x16_bf16 v[136:151], v[96:99], %[q2], 0\n\t" \
  "v_exp_f32 v73, v73\n\t" \
  "ds_read_b128 v[96:99], %[ka] offset:16384\n\t" \
  "v_exp_f32 v74, v74\n\t" \
  "v_exp_f32 v75, v75\n\t" \
  "v_exp_f32 v76, v76\n\t" \
  "v_exp_f32 v77, v77\n\t" \
  "s_waitcnt lgkmcnt(2)\n\t" \
  "v_smfmac_f32_32x32x32_bf16 %[o], v[104:107], v[64:71], %[idx]\n\t" \
  "v_exp_f32 v78, v78\n\t" \
  "v_exp_f32 v79, v79\n\t" \
  "v_exp_f32 v128, v128\n\t" \
  "v_exp_f32 v129, v129\n\t" \
  "v_exp_f32 v130, v130\n\t" \
  "v_smfmac_f32_32x32x32_bf16 %[o2], v[104:107], v[120:127], %[idx]\n\t" \
  "v_exp_f32 v131, v131\n\t" \
  "ds_read_b128 v[104:107], %[va] offset:3072\n\t" \
  "v_exp_f32 v132, v132\n\t" \
  "v_exp_f32 v133, v133\n\t" \
  "v_exp_f32 v134, v134\n\t" \
  "v_exp_f32 v135, v135\n\t" \
  "v_exp_f32 v80, v80\n\t" \
  "v_exp_f32 v81, v81\n\t" \
  "s_waitcnt lgkmcnt(2)\n\t" \
  "v_smfmac_f32_32x32x32_bf16 %[o], v[108:111], v[72:79], %[idx]\n\t" \
  "v_exp_f32 v82, v82\n\t" \
  "v_exp_f32 v83, v83\n\t" \
  "v_exp_f32 v84, v84\n\t" \
  "v_exp_f32 v85, v85\n\t" \
  "v_exp_f32 v86, v86\n\t" \
  "v_smfmac_f32_32x32x32_bf16 %[o2], v[108:111], v[128:135], %[idx]\n\t" \
  "v_exp_f32 v87, v87\n\t" \
  "ds_read_b128 v[108:111], %[va] offset:3584\n\t" \
  "v_exp_f32 v136, v136\n\t" \
  "v_exp_f32 v137, v137\n\t" \
  "v_exp_f32 v138, v138\n\t" \
  "v_exp_f32 v139, v139\n\t" \
  "s_waitcnt lgkmcnt(2)\n\t" \
  "v_mfma_f32_32x32x16_bf16 v[64:79], v[96:99], %[q], 0\n\t" \
  "v_exp_f32 v140, v140\n\t" \
  "v_exp_f32 v141, v141\n\t" \
  "v_exp_f32 v142, v142\n\t" \
  "v_exp_f32 v143, v143\n\t" \
  "v_exp_f32 v88, v88\n\t" \
  "v_mfma_f32_32x32x16_bf16 v[120:135], v[96:99], %[q2], 0\n\t" \
  "v_exp_f32 v89, v89\n\t" \
  "ds_read_b128 v[96:99], %[ka] offset:17408\n\t" \
  "v_exp_f32 v90, v90\n\t" \
  "v_exp_f32 v91, v91\n\t" \
  "v_exp_f32 v92, v92\n\t" \
  "v_exp_f32 v93, v93\n\t" \
  "s_waitcnt lgkmcnt(2)\n\t" \
  "v_smfmac_f32_32x32x32_bf16 %[o], v[104:107], v[80:87], %[idx]\n\t" \
  "v_exp_f32 v94, v94\n\t" \
  "v_exp_f32 v95, v95\n\t" \
  "v_exp_f32 v144, v144\n\t" \
  "v_exp_f32 v145, v145\n\t" \
  "v_exp_f32 v146, v146\n\t" \
  "v_smfmac_f32_32x32x32_bf16 %[o2], v[104:107], v[136:143], %[idx]\n\t" \
  "v_exp_f32 v147, v147\n\t" \
  "v_add_u32 %[va], %[vinc], %[va]\n\t" \
  "ds_read_b128 v[104:107], %[va]\n\t" \
  "v_exp_f32 v148, v148\n\t" \
  "v_exp_f32 v149, v149\n\t" \
  "v_exp_f32 v150, v150\n\t" \
  "v_exp_f32 v151, v151\n\t" \
  "v_add_u32 %[ka], %[kinc], %[ka]\n\t" \
  "v_exp_f32 v64, v64\n\t" \
  "v_exp_f32 v65, v65\n\t" \
  "s_waitcnt lgkmcnt(2)\n\t" \
  "v_smfmac_f32_32x32x32_bf16 %[o], v[108:111], v[88:95], %[idx]\n\t" \
  "v_exp_f32 v66, v66\n\t" \
  "v_exp_f32 v67, v67\n\t" \
  "global_load_dwordx4 v[168:171], %[wofs], %[wo]\n\t" \
  "v_add_u32 %[wofs], 0x10000, %[wofs]\n\t" \
  "v_exp_f32 v68, v68\n\t" \
  "v_exp_f32 v69, v69\n\t" \
  "v_exp_f32 v70, v70\n\t" \
  "v_smfmac_f32_32x32x32_bf16 %[o2], v[108:111], v[144:151], %[idx]\n\t" \
  "v_exp_f32 v71, v71\n\t" \
  "ds_read_b128 v[108:111], %[va] offset:512\n\t" \
  "v_exp_f32 v120, v120\n\t" \
  "v_exp_f32 v121, v121\n\t" \
  "v_exp_f32 v122, v122\n\t" \
  "v_exp_f32 v123, v123\n\t" \
  "s_waitcnt lgkmcnt(2)\n\t" \
  "v_mfma_f32_32x32x16_bf16 v[80:95], v[96:99], %[q], 0\n\t" \
  "v_exp_f32 v124, v124\n\t" \
  "v_exp_f32 v125, v125\n\t" \
  "v_exp_f32 v126, v126\n\t" \
  "v_exp_f32 v127, v127\n\t" \
  "v_exp_f32 v72, v72\n\t" \
  "v_mfma_f32_32x32x16_bf16 v[136:151], v[96:99], %[q2], 0\n\t" \
  "v_exp_f32 v73, v73\n\t" \
  "ds_read_b128 v[96:99], %[ka] offset:2048\n\t" \
  "v_exp_f32 v74, v74\n\t" \
  "v_exp_f32 v75, v75\n\t" \
  "v_exp_f32 v76, v76\n\t" \
  "v_exp_f32 v77, v77\n\t" \
  "s_waitcnt lgkmcnt(2)\n\t" \
  "v_smfmac_f32_32x32x32_bf16 %[o], v[104:107], v[64:71], %[idx]\n\t" \
  "v_exp_f32 v78, v78\n\t" \
  "v_exp_f32 v79, v79\n\t" \
  "v_exp_f32 v128, v128\n\t" \
  "v_exp_f32 v129, v129\n\t" \
  "v_exp_f32 v130, v130\n\t" \
  "v_smfmac_f32_32x32x32_bf16 %[o2], v[104:107], v[120:127], %[idx]\n\t" \
  "v_exp_f32 v131, v131\n\t" \
  "ds_read_b128 v[104:107], %[va] offset:1024\n\t" \
  "v_exp_f32 v132, v132\n\t" \
  "v_exp_f32 v133, v133\n\t" \
  "v_exp_f32 v134, v134\n\t" \
  "v_exp_f32 v135, v135\n\t" \
  "v_exp_f32 v80, v80\n\t" \
  "v_exp_f32 v81, v81\n\t" \
  "s_waitcnt lgkmcnt(2)\n\t" \
  "v_smfmac_f32_32x32x32_bf16 %[o], v[108:111], v[72:79], %[idx]\n\t" \
  "v_exp_f32 v82, v82\n\t" \
  "v_exp_f32 v83, v83\n\t" \
  "v_exp_f32 v84, v84\n\t" \
  "v_exp_f32 v85, v85\n\t" \
  "v_exp_f32 v86, v86\n\t" \
  "v_smfmac_f32_32x32x32_bf16 %[o2], v[108:111], v[128:135], %[idx]\n\t" \
  "v_exp_f32 v87, v87\n\t" \
  "ds_read_b128 v[108:111], %[va] offset:1536\n\t" \
  "v_exp_f32 v136, v136\n\t" \
  "v_exp_f32 v137, v137\n\t" \
  "v_exp_f32 v138, v138\n\t" \
  "v_exp_f32 v139, v139\n\t" \
  "s_waitcnt lgkmcnt(2)\n\t" \
  "v_mfma_f32_32x32x16_bf16 v[64:79], v[96:99], %[q], 0\n\t" \
  "v_exp_f32 v140, v140\n\t" \
  "v_exp_f32 v141, v141\n\t" \
  "v_exp_f32 v142, v142\n\t" \
  "v_exp_f32 v143, v143\n\t" \
  "v_exp_f32 v88, v88\n\t" \
  "v_mfma_f32_32x32x16_bf16 v[120:135], v[96:99], %[q2], 0\n\t" \
  "v_exp_f32 v89, v89\n\t" \
  "ds_read_b128 v[96:99], %[ka] offset:3072\n\t" \
  "v_exp_f32 v90, v90\n\t" \
  "v_exp_f32 v91, v91\n\t" \
  "v_exp_f32 v92, v92\n\t" \
  "v_exp_f32 v93, v93\n\t" \
  "s_waitcnt lgkmcnt(2)\n\t" \
  "v_smfmac_f32_32x32x32_bf16 %[o], v[104:107], v[80:87], %[idx]\n\t" \
  "v_exp_f32 v94, v94\n\t" \
  "v_exp_f32 v95, v95\n\t" \
  "v_exp_f32 v144, v144\n\t" \
  "v_exp_f32 v145, v145\n\t" \
  "v_exp_f32 v146, v146\n\t" \
  "v_smfmac_f32_32x32x32_bf16 %[o2], v[104:107], v[136:143], %[idx]\n\t" \
  "v_exp_f32 v147, v147\n\t" \
  "ds_read_b128 v[104:107], %[va] offset:2048\n\t" \
  "v_exp_f32 v148, v148\n\t" \
  "v_exp_f32 v149, v149\n\t" \
  "v_exp_f32 v150, v150\n\t" \
  "v_exp_f32 v151, v151\n\t" \
  "v_exp_f32 v64, v64\n\t" \
  "v_exp_f32 v65, v65\n\t" \
  "s_waitcnt lgkmcnt(2)\n\t" \
  "v_smfmac_f32_32x32x32_bf16 %[o], v[108:111], v[88:95], %[idx]\n\t" \
  "v_exp_f32 v66, v66\n\t" \
  "v_exp_f32 v67, v67\n\t" \
  "global_load_dword v194, %[dofs], %[wd]\n\t" \
  "v_exp_f32 v68, v68\n\t" \
  "v_exp_f32 v69, v69\n\t" \
  "v_exp_f32 v70, v70\n\t" \
  "v_smfmac_f32_32x32x32_bf16 %[o2], v[108:111], v[144:151], %[idx]\n\t" \
  "v_exp_f32 v71, v71\n\t" \
  "ds_read_b128 v[108:111], %[va] offset:2560\n\t" \
  "v_exp_f32 v120, v120\n\t" \
  "v_exp_f32 v121, v121\n\t" \
  "v_exp_f32 v122, v122\n\t" \
  "v_exp_f32 v123, v123\n\t" \
  "s_waitcnt lgkmcnt(2)\n\t" \
  "v_mfma_f32_32x32x16_bf16 v[80:95], v[96:99], %[q], 0\n\t" \
  "v_exp_f32 v124, v124\n\t" \
  "v_exp_f32 v125, v125\n\t" \
  "v_exp_f32 v126, v126\n\t" \
  "v_exp_f32 v127, v127\n\t" \
  "v_exp_f32 v72, v72\n\t" \
  "v_mfma_f32_32x32x16_bf16 v[136:151], v[96:99], %[q2], 0\n\t" \
  "v_exp_f32 v73, v73\n\t" \
  "ds_read_b128 v[96:99], %[ka] offset:16384\n\t" \
  "v_exp_f32 v74, v74\n\t" \
  "v_exp_f32 v75, v75\n\t" \
  "v_exp_f32 v76, v76\n\t" \
  "v_exp_f32 v77, v77\n\t" \
  "s_waitcnt lgkmcnt(2)\n\t" \
  "v_smfmac_f32_32x32x32_bf16 %[o], v[104:107], v[64:71], %[idx]\n\t" \
  "v_exp_f32 v78, v78\n\t" \
  "v_exp_f32 v79, v79\n\t" \
  "v_exp_f32 v128, v128\n\t" \
  "v_exp_f32 v129, v129\n\t" \
  "v_exp_f32 v130, v130\n\t" \
  "v_smfmac_f32_32x32x32_bf16 %[o2], v[104:107], v[120:127], %[idx]\n\t" \
  "v_exp_f32 v131, v131\n\t" \
  "ds_read_b128 v[104:107], %[va] offset:3072\n\t" \
  "v_exp_f32 v132, v132\n\t" \
  "v_exp_f32 v133, v133\n\t" \
  "v_exp_f32 v134, v134\n\t" \
  "v_exp_f32 v135, v135\n\t" \
  "v_exp_f32 v80, v80\n\t" \
  "v_exp_f32 v81, v81\n\t" \
  "s_waitcnt lgkmcnt(2)\n\t" \
  "v_smfmac_f32_32x32x32_bf16 %[o], v[108:111], v[72:79], %[idx]\n\t" \
  "v_exp_f32 v82, v82\n\t" \
  "v_exp_f32 v83, v83\n\t" \
  "v_exp_f32 v84, v84\n\t" \
  "v_exp_f32 v85, v85\n\t" \
  "v_exp_f32 v86, v86\n\t" \
  "v_smfmac_f32_32x32x32_bf16 %[o2], v[108:111], v[128:135], %[idx]\n\t" \
  "v_exp_f32 v87, v87\n\t" \
  "ds_read_b128 v[108:111], %[va] offset:3584\n\t" \
  "v_exp_f32 v136, v136\n\t" \
  "v_exp_f32 v137, v137\n\t" \
  "v_exp_f32 v138, v138\n\t" \
  "v_exp_f32 v139, v139\n\t" \
  "s_waitcnt lgkmcnt(2)\n\t" \
  "v_mfma_f32_32x32x16_bf16 v[64:79], v[96:99], %[q], 0\n\t" \
  "v_exp_f32 v140, v140\n\t" \
  "v_exp_f32 v141, v141\n\t" \
  "v_exp_f32 v142, v142\n\t" \
  "v_exp_f32 v143, v143\n\t" \
  "v_exp_f32 v88, v88\n\t" \
  "v_mfma_f32_32x32x16_bf16 v[120:135], v[96:99], %[q2], 0\n\t" \
  "v_exp_f32 v89, v89\n\t" \
  "ds_read_b128 v[96:99], %[ka] offset:17408\n\t" \
  "v_exp_f32 v90, v90\n\t" \
  "v_exp_f32 v91, v91\n\t" \
  "v_exp_f32 v92, v92\n\t" \
  "v_exp_f32 v93, v93\n\t" \
  "s_waitcnt lgkmcnt(2)\n\t" \
  "v_smfmac_f32_32x32x32_bf16 %[o], v[104:107], v[80:87], %[idx]\n\t" \
  "v_exp_f32 v94, v94\n\t" \
  "v_exp_f32 v95, v95\n\t" \
  "v_exp_f32 v144, v144\n\t" \
  "v_exp_f32 v145, v145\n\t" \
  "v_exp_f32 v146, v146\n\t" \
  "v_smfmac_f32_32x32x32_bf16 %[o2], v[104:107], v[136:143], %[idx]\n\t" \
  "v_exp_f32 v147, v147\n\t" \
  "v_add_u32 %[va], %[vinc], %[va]\n\t" \
  "ds_read_b128 v[104:107], %[va]\n\t" \
  "v_exp_f32 v148, v148\n\t" \
  "v_exp_f32 v149, v149\n\t" \
  "v_exp_f32 v150, v150\n\t" \
  "v_exp_f32 v151, v151\n\t" \
  "v_add_u32 %[ka], %[kinc], %[ka]\n\t" \
  "v_exp_f32 v64, v64\n\t" \
  "v_exp_f32 v65, v65\n\t" \
  "s_waitcnt lgkmcnt(2)\n\t" \
  "v_smfmac_f32_32x32x32_bf16 %[o], v[108:111], v[88:95], %[idx]\n\t" \
  "v_exp_f32 v66, v66\n\t" \
  "v_exp_f32 v67, v67\n\t" \
  "global_load_dwordx4 v[172:175], %[wofs], %[wo]\n\t" \
  "v_add_u32 %[wofs], 0x10000, %[wofs]\n\t" \
  "v_exp_f32 v68, v68\n\t" \
  "v_exp_f32 v69, v69\n\t" \
  "v_exp_f32 v70, v70\n\t" \
  "v_smfmac_f32_32x32x32_bf16 %[o2], v[108:111], v[144:151], %[idx]\n\t" \
  "v_exp_f32 v71, v71\n\t" \
  "ds_read_b128 v[108:111], %[va] offset:512\n\t" \
  "v_exp_f32 v120, v120\n\t" \
  "v_exp_f32 v121, v121\n\t" \
  "v_exp_f32 v122, v122\n\t" \
  "v_exp_f32 v123, v123\n\t" \
  "s_waitcnt lgkmcnt(2)\n\t" \
  "v_mfma_f32_32x32x16_bf16 v[80:95], v[96:99], %[q], 0\n\t" \
  "v_exp_f32 v124, v124\n\t" \
  "v_exp_f32 v125, v125\n\t" \
  "v_exp_f32 v126, v126\n\t" \
  "v_exp_f32 v127, v127\n\t" \
  "v_exp_f32 v72, v72\n\t" \
  "v_mfma_f32_32x32x16_bf16 v[136:151], v[96:99], %[q2], 0\n\t" \
  "v_exp_f32 v73, v73\n\t" \
  "ds_read_b128 v[96:99], %[ka] offset:2048\n\t" \
  "v_exp_f32 v74, v74\n\t" \
  "v_exp_f32 v75, v75\n\t" \
  "v_exp_f32 v76, v76\n\t" \
  "v_exp_f32 v77, v77\n\t" \
  "s_waitcnt lgkmcnt(2)\n\t" \
  "v_smfmac_f32_32x32x32_bf16 %[o], v[104:107], v[64:71], %[idx]\n\t" \
  "v_exp_f32 v78, v78\n\t" \
  "v_exp_f32 v79, v79\n\t" \
  "v_exp_f32 v128, v128\n\t" \
  "v_exp_f32 v129, v129\n\t" \
  "v_exp_f32 v130, v130\n\t" \
  "v_smfmac_f32_32x32x32_bf16 %[o2], v[104:107], v[120:127], %[idx]\n\t" \
  "v_exp_f32 v131, v131\n\t" \
  "ds_read_b128 v[104:107], %[va] offset:1024\n\t" \
  "v_exp_f32 v132, v132\n\t" \
  "v_exp_f32 v133, v133\n\t" \
  "v_exp_f32 v134, v134\n\t" \
  "v_exp_f32 v135, v135\n\t" \
  "v_exp_f32 v80, v80\n\t" \
  "v_exp_f32 v81, v81\n\t" \
  "s_waitcnt lgkmcnt(2)\n\t" \
  "v_smfmac_f32_32x32x32_bf16 %[o], v[108:111], v[72:79], %[idx]\n\t" \
  "v_exp_f32 v82, v82\n\t" \
  "v_exp_f32 v83, v83\n\t" \
  "v_exp_f32 v84, v84\n\t" \
  "v_exp_f32 v85, v85\n\t" \
  "v_exp_f32 v86, v86\n\t" \
  "v_smfmac_f32_32x32x32_bf16 %[o2], v[108:111], v[128:135], %[idx]\n\t" \
  "v_exp_f32 v87, v87\n\t" \
  "ds_read_b128 v[108:111], %[va] offset:1536\n\t" \
  "v_exp_f32 v136, v136\n\t" \
  "v_exp_f32 v137, v137\n\t" \
  "v_exp_f32 v138, v138\n\t" \
  "v_exp_f32 v139, v139\n\t" \
  "s_waitcnt lgkmcnt(2)\n\t" \
  "v_mfma_f32_32x32x16_bf16 v[64:79], v[96:99], %[q], 0\n\t" \
  "v_exp_f32 v140, v140\n\t" \
  "v_exp_f32 v141, v141\n\t" \
  "v_exp_f32 v142, v142\n\t" \
  "v_exp_f32 v143, v143\n\t" \
  "v_exp_f32 v88, v88\n\t" \
  "v_mfma_f32_32x32x16_bf16 v[120:135], v[96:99], %[q2], 0\n\t" \
  "v_exp_f32 v89, v89\n\t" \
  "ds_read_b128 v[96:99], %[ka] offset:3072\n\t" \
  "v_exp_f32 v90, v90\n\t" \
  "v_exp_f32 v91, v91\n\t" \
  "v_exp_f32 v92, v92\n\t" \
  "v_exp_f32 v93, v93\n\t" \
  "s_waitcnt lgkmcnt(2)\n\t" \
  "v_smfmac_f32_32x32x32_bf16 %[o], v[104:107], v[80:87], %[idx]\n\t" \
  "v_exp_f32 v94, v94\n\t" \
  "v_exp_f32 v95, v95\n\t" \
  "v_exp_f32 v144, v144\n\t" \
  "v_exp_f32 v145, v145\n\t" \
  "v_exp_f32 v146, v146\n\t" \
  "v_smfmac_f32_32x32x32_bf16 %[o2], v[104:107], v[136:143], %[idx]\n\t" \
  "v_exp_f32 v147, v147\n\t" \
  "ds_read_b128 v[104:107], %[va] offset:2048\n\t" \
  "v_exp_f32 v148, v148\n\t" \
  "v_exp_f32 v149, v149\n\t" \
  "v_exp_f32 v150, v150\n\t" \
  "v_exp_f32 v151, v151\n\t" \
  "v_exp_f32 v64, v64\n\t" \
  "v_exp_f32 v65, v65\n\t" \
  "s_waitcnt lgkmcnt(2)\n\t" \
  "v_smfmac_f32_32x32x32_bf16 %[o], v[108:111], v[88:95], %[idx]\n\t" \
  "v_exp_f32 v66, v66\n\t" \
  "v_exp_f32 v67, v67\n\t" \
  "global_load_dword v195, %[dofs3], %[wd]\n\t" \
  "v_exp_f32 v68, v68\n\t" \
  "v_exp_f32 v69, v69\n\t" \
  "v_exp_f32 v70, v70\n\t" \
  "v_smfmac_f32_32x32x32_bf16 %[o2], v[108:111], v[144:151], %[idx]\n\t" \
  "v_exp_f32 v71, v71\n\t" \
  "ds_read_b128 v[108:111], %[va] offset:2560\n\t" \
  "v_exp_f32 v120, v120\n\t" \
  "v_exp_f32 v121, v121\n\t" \
  "v_exp_f32 v122, v122\n\t" \
  "v_exp_f32 v123, v123\n\t" \
  "s_waitcnt lgkmcnt(2)\n\t" \
  "v_mfma_f32_32x32x16_bf16 v[80:95], v[96:99], %[q], 0\n\t" \
  "v_exp_f32 v124, v124\n\t" \
  "v_exp_f32 v125, v125\n\t" \
  "v_exp_f32 v126, v126\n\t" \
  "v_exp_f32 v127, v127\n\t" \
  "v_exp_f32 v72, v72\n\t" \
  "v_mfma_f32_32x32x16_bf16 v[136:151], v[96:99], %[q2], 0\n\t" \
  "v_exp_f32 v73, v73\n\t" \
  "ds_read_b128 v[96:99], %[ka] offset:16384\n\t" \
  "v_exp_f32 v74, v74\n\t" \
  "v_exp_f32 v75, v75\n\t" \
  "v_exp_f32 v76, v76\n\t" \
  "v_exp_f32 v77, v77\n\t" \
  "s_waitcnt lgkmcnt(2)\n\t" \
  "v_smfmac_f32_32x32x32_bf16 %[o], v[104:107], v[64:71], %[idx]\n\t" \
  "v_exp_f32 v78, v78\n\t" \
  "v_exp_f32 v79, v79\n\t" \
  "v_exp_f32 v128, v128\n\t" \
  "v_exp_f32 v129, v129\n\t" \
  "v_exp_f32 v130, v130\n\t" \
  "v_smfmac_f32_32x32x32_bf16 %[o2], v[104:107], v[120:127], %[idx]\n\t" \
  "v_exp_f32 v131, v131\n\t" \
  "ds_read_b128 v[104:107], %[va] offset:3072\n\t" \
  "v_exp_f32 v132, v132\n\t" \
  "v_exp_f32 v133, v133\n\t" \
  "v_exp_f32 v134, v134\n\t" \
  "v_exp_f32 v135, v135\n\t" \
  "v_exp_f32 v80, v80\n\t" \
  "v_exp_f32 v81, v81\n\t" \
  "s_waitcnt lgkmcnt(2)\n\t" \
  "v_smfmac_f32_32x32x32_bf16 %[o], v[108:111], v[72:79], %[idx]\n\t" \
  "v_exp_f32 v82, v82\n\t" \
  "v_exp_f32 v83, v83\n\t" \
  "v_exp_f32 v84, v84\n\t" \
  "v_exp_f32 v85, v85\n\t" \
  "v_exp_f32 v86, v86\n\t" \
  "v_smfmac_f32_32x32x32_bf16 %[o2], v[108:111], v[128:135], %[idx]\n\t" \
  "v_exp_f32 v87, v87\n\t" \
  "ds_read_b128 v[108:111], %[va] offset:3584\n\t" \
  "v_exp_f32 v136, v136\n\t" \
  "v_exp_f32 v137, v137\n\t" \
  "v_exp_f32 v138, v138\n\t" \
  "v_exp_f32 v139, v139\n\t" \
  "s_waitcnt lgkmcnt(2)\n\t" \
  "v_mfma_f32_32x32x16_bf16 v[64:79], v[96:99], %[q], 0\n\t" \
  "v_exp_f32 v140, v140\n\t" \
  "v_exp_f32 v141, v141\n\t" \
  "v_exp_f32 v142, v142\n\t" \
  "v_exp_f32 v143, v143\n\t" \
  "v_exp_f32 v88, v88\n\t" \
  "v_mfma_f32_32x32x16_bf16 v[120:135], v[96:99], %[q2], 0\n\t" \
  "v_exp_f32 v89, v89\n\t" \
  "ds_read_b128 v[96:99], %[ka] offset:17408\n\t" \
  "v_exp_f32 v90, v90\n\t" \
  "v_exp_f32 v91, v91\n\t" \
  "v_exp_f32 v92, v92\n\t" \
  "v_exp_f32 v93, v93\n\t" \
  "s_waitcnt lgkmcnt(2)\n\t" \
  "v_smfmac_f32_32x32x32_bf16 %[o], v[104:107], v[80:87], %[idx]\n\t" \
  "v_exp_f32 v94, v94\n\t" \
  "v_exp_f32 v95, v95\n\t" \
  "v_exp_f32 v144, v144\n\t" \
  "v_exp_f32 v145, v145\n\t" \
  "v_exp_f32 v146, v146\n\t" \
  "v_smfmac_f32_32x32x32_bf16 %[o2], v[104:107], v[136:143], %[idx]\n\t" \
  "v_exp_f32 v147, v147\n\t" \
  "v_add_u32 %[va], %[vinc], %[va]\n\t" \
  "ds_read_b128 v[104:107], %[va]\n\t" \
  "v_exp_f32 v148, v148\n\t" \
  "v_exp_f32 v149, v149\n\t" \
  "v_exp_f32 v150, v150\n\t" \
  "v_exp_f32 v151, v151\n\t" \
  "v_add_u32 %[ka], %[kinc], %[ka]\n\t" \
  "v_exp_f32 v64, v64\n\t" \
  "v_exp_f32 v65, v65\n\t" \
  "s_waitcnt lgkmcnt(2)\n\t" \
  "v_smfmac_f32_32x32x32_bf16 %[o], v[108:111], v[88:95], %[idx]\n\t" \
  "v_exp_f32 v66, v66\n\t" \
  "v_exp_f32 v67, v67\n\t" \
  "global_load_dwordx4 v[176:179], %[wofs], %[wo]\n\t" \
  "v_add_u32 %[wofs], 0x10000, %[wofs]\n\t" \
  "v_exp_f32 v68, v68\n\t" \
  "v_exp_f32 v69, v69\n\t" \
  "v_exp_f32 v70, v70\n\t" \
  "v_smfmac_f32_32x32x32_bf16 %[o2], v[108:111], v[144:151], %[idx]\n\t" \
  "v_exp_f32 v71, v71\n\t" \
  "ds_read_b128 v[108:111], %[va] offset:512\n\t" \
  "v_exp_f32 v120, v120\n\t" \
  "v_exp_f32 v121, v121\n\t" \
  "v_exp_f32 v122, v122\n\t" \
  "v_exp_f32 v123, v123\n\t" \
  "s_waitcnt lgkmcnt(2)\n\t" \
  "v_mfma_f32_32x32x16_bf16 v[80:95], v[96:99], %[q], 0\n\t" \
  "v_exp_f32 v124, v124\n\t" \
  "v_exp_f32 v125, v125\n\t" \
  "v_exp_f32 v126, v126\n\t" \
  "v_exp_f32 v127, v127\n\t" \
  "v_exp_f32 v72, v72\n\t" \
  "v_mfma_f32_32x32x16_bf16 v[136:151], v[96:99], %[q2], 0\n\t" \
  "v_exp_f32 v73, v73\n\t" \
  "ds_read_b128 v[96:99], %[ka] offset:2048\n\t" \
  "v_exp_f32 v74, v74\n\t" \
  "v_exp_f32 v75, v75\n\t" \
  "v_exp_f32 v76, v76\n\t" \
  "v_exp_f32 v77, v77\n\t" \
  "s_waitcnt lgkmcnt(2)\n\t" \
  "v_smfmac_f32_32x32x32_bf16 %[o], v[104:107], v[64:71], %[idx]\n\t" \
  "v_exp_f32 v78, v78\n\t" \
  "v_exp_f32 v79, v79\n\t" \
  "v_exp_f32 v128, v128\n\t" \
  "v_exp_f32 v129, v129\n\t" \
  "v_exp_f32 v130, v130\n\t" \
  "v_smfmac_f32_32x32x32_bf16 %[o2], v[104:107], v[120:127], %[idx]\n\t" \
  "v_exp_f32 v131, v131\n\t" \
  "ds_read_b128 v[104:107], %[va] offset:1024\n\t" \
  "v_exp_f32 v132, v132\n\t" \
  "v_exp_f32 v133, v133\n\t" \
  "v_exp_f32 v134, v134\n\t" \
  "v_exp_f32 v135, v135\n\t" \
  "v_exp_f32 v80, v80\n\t" \
  "v_exp_f32 v81, v81\n\t" \
  "s_waitcnt lgkmcnt(2)\n\t" \
  "v_smfmac_f32_32x32x32_bf16 %[o], v[108:111], v[72:79], %[idx]\n\t" \
  "v_exp_f32 v82, v82\n\t" \
  "v_exp_f32 v83, v83\n\t" \
  "v_exp_f32 v84, v84\n\t" \
  "v_exp_f32 v85, v85\n\t" \
  "v_exp_f32 v86, v86\n\t" \
  "v_smfmac_f32_32x32x32_bf16 %[o2], v[108:111], v[128:135], %[idx]\n\t" \
  "v_exp_f32 v87, v87\n\t" \
  "ds_read_b128 v[108:111], %[va] offset:1536\n\t" \
  "v_exp_f32 v136, v136\n\t" \
  "v_exp_f32 v137, v137\n\t" \
  "v_exp_f32 v138, v138\n\t" \
  "v_exp_f32 v139, v139\n\t" \
  "s_waitcnt lgkmcnt(2)\n\t" \
  "v_mfma_f32_32x32x16_bf16 v[64:79], v[96:99], %[q], 0\n\t" \
  "v_exp_f32 v140, v140\n\t" \
  "v_exp_f32 v141, v141\n\t" \
  "v_exp_f32 v142, v142\n\t" \
  "v_exp_f32 v143, v143\n\t" \
  "v_exp_f32 v88, v88\n\t" \
  "v_mfma_f32_32x32x16_bf16 v[120:135], v[96:99], %[q2], 0\n\t" \
  "v_exp_f32 v89, v89\n\t" \
  "ds_read_b128 v[96:99], %[ka] offset:3072\n\t" \
  "v_exp_f32 v90, v90\n\t" \
  "v_exp_f32 v91, v91\n\t" \
  "v_exp_f32 v92, v92\n\t" \
  "v_exp_f32 v93, v93\n\t" \
  "s_waitcnt lgkmcnt(2)\n\t" \
  "v_smfmac_f32_32x32x32_bf16 %[o], v[104:107], v[80:87], %[idx]\n\t" \
  "v_exp_f32 v94, v94\n\t" \
  "v_exp_f32 v95, v95\n\t" \
  "v_exp_f32 v144, v144\n\t" \
  "v_exp_f32 v145, v145\n\t" \
  "v_exp_f32 v146, v146\n\t" \
  "v_smfmac_f32_32x32x32_bf16 %[o2], v[104:107], v[136:143], %[idx]\n\t" \
  "v_exp_f32 v147, v147\n\t" \
  "ds_read_b128 v[104:107], %[va] offset:2048\n\t" \
  "v_exp_f32 v148, v148\n\t" \
  "v_exp_f32 v149, v149\n\t" \
  "v_exp_f32 v150, v150\n\t" \
  "v_exp_f32 v151, v151\n\t" \
  "v_exp_f32 v64, v64\n\t" \
  "v_exp_f32 v65, v65\n\t" \
  "s_waitcnt lgkmcnt(2)\n\t" \
  "v_smfmac_f32_32x32x32_bf16 %[o], v[108:111], v[88:95], %[idx]\n\t" \
  "v_exp_f32 v66, v66\n\t" \
  "v_exp_f32 v67, v67\n\t" \
  "global_load_dwordx4 v[180:183], %[wofs], %[wo]\n\t" \
  "v_add_u32 %[wofs], 0x10000, %[wofs]\n\t" \
  "v_exp_f32 v68, v68\n\t" \
  "v_exp_f32 v69, v69\n\t" \
  "v_exp_f32 v70, v70\n\t" \
  "v_smfmac_f32_32x32x32_bf16 %[o2], v[108:111], v[144:151], %[idx]\n\t" \
  "v_exp_f32 v71, v71\n\t" \
  "ds_read_b128 v[108:111], %[va] offset:2560\n\t" \
  "v_exp_f32 v120, v120\n\t" \
  "v_exp_f32 v121, v121\n\t" \
  "v_exp_f32 v122, v122\n\t" \
  "v_exp_f32 v123, v123\n\t" \
  "s_waitcnt lgkmcnt(2)\n\t" \
  "v_mfma_f32_32x32x16_bf16 v[80:95], v[96:99], %[q], 0\n\t" \
  "v_exp_f32 v124, v124\n\t" \
  "v_exp_f32 v125, v125\n\t" \
  "v_exp_f32 v126, v126\n\t" \
  "v_exp_f32 v127, v127\n\t" \
  "v_exp_f32 v72, v72\n\t" \
  "v_mfma_f32_32x32x16_bf16 v[136:151], v[96:99], %[q2], 0\n\t" \
  "v_exp_f32 v73, v73\n\t" \
  "ds_read_b128 v[96:99], %[ka] offset:16384\n\t" \
  "v_exp_f32 v74, v74\n\t" \
  "v_exp_f32 v75, v75\n\t" \
  "v_exp_f32 v76, v76\n\t" \
  "v_exp_f32 v77, v77\n\t" \
  "s_waitcnt lgkmcnt(2)\n\t" \
  "v_smfmac_f32_32x32x32_bf16 %[o], v[104:107], v[64:71], %[idx]\n\t" \
  "v_exp_f32 v78, v78\n\t" \
  "v_exp_f32 v79, v79\n\t" \
  "v_exp_f32 v128, v128\n\t" \
  "v_exp_f32 v129, v129\n\t" \
  "v_exp_f32 v130, v130\n\t" \
  "v_smfmac_f32_32x32x32_bf16 %[o2], v[104:107], v[120:127], %[idx]\n\t" \
  "v_exp_f32 v131, v131\n\t" \
  "ds_read_b128 v[104:107], %[va] offset:3072\n\t" \
  "v_exp_f32 v132, v132\n\t" \
  "v_exp_f32 v133, v133\n\t" \
  "v_exp_f32 v134, v134\n\t" \
  "v_exp_f32 v135, v135\n\t" \
  "v_exp_f32 v80, v80\n\t" \
  "v_exp_f32 v81, v81\n\t" \
  "s_waitcnt lgkmcnt(2)\n\t" \
  "v_smfmac_f32_32x32x32_bf16 %[o], v[108:111], v[72:79], %[idx]\n\t" \
  "v_exp_f32 v82, v82\n\t" \
  "v_exp_f32 v83, v83\n\t" \
  "v_exp_f32 v84, v84\n\t" \
  "v_exp_f32 v85, v85\n\t" \
  "v_exp_f32 v86, v86\n\t" \
  "v_smfmac_f32_32x32x32_bf16 %[o2], v[108:111], v[128:135], %[idx]\n\t" \
  "v_exp_f32 v87, v87\n\t" \
  "ds_read_b128 v[108:111], %[va] offset:3584\n\t" \
  "v_exp_f32 v136, v136\n\t" \
  "v_exp_f32 v137, v137\n\t" \
  "v_exp_f32 v138, v138\n\t" \
  "v_exp_f32 v139, v139\n\t" \
  "s_waitcnt lgkmcnt(2)\n\t" \
  "v_mfma_f32_32x32x16_bf16 v[64:79], v[96:99], %[q], 0\n\t" \
  "v_exp_f32 v140, v140\n\t" \
  "v_exp_f32 v141, v141\n\t" \
  "v_exp_f32 v142, v142\n\t" \
  "v_exp_f32 v143, v143\n\t" \
  "v_exp_f32 v88, v88\n\t" \
  "v_mfma_f32_32x32x16_bf16 v[120:135], v[96:99], %[q2], 0\n\t" \
  "v_exp_f32 v89, v89\n\t" \
  "ds_read_b128 v[96:99], %[ka] offset:17408\n\t" \
  "v_exp_f32 v90, v90\n\t" \
  "v_exp_f32 v91, v91\n\t" \
  "v_exp_f32 v92, v92\n\t" \
  "v_exp_f32 v93, v93\n\t" \
  "s_waitcnt lgkmcnt(2)\n\t" \
  "v_smfmac_f32_32x32x32_bf16 %[o], v[104:107], v[80:87], %[idx]\n\t" \
  "v_exp_f32 v94, v94\n\t" \
  "v_exp_f32 v95, v95\n\t" \
  "v_exp_f32 v144, v144\n\t" \
  "v_exp_f32 v145, v145\n\t" \
  "v_exp_f32 v146, v146\n\t" \
  "v_smfmac_f32_32x32x32_bf16 %[o2], v[104:107], v[136:143], %[idx]\n\t" \
  "v_exp_f32 v147, v147\n\t" \
  "v_add_u32 %[va], %[vinc], %[va]\n\t" \
  "ds_read_b128 v[104:107], %[va]\n\t" \
  "v_exp_f32 v148, v148\n\t" \
  "v_exp_f32 v149, v149\n\t" \
  "v_exp_f32 v150, v150\n\t" \
  "v_exp_f32 v151, v151\n\t" \
  "v_add_u32 %[ka], %[kinc], %[ka]\n\t" \
  "s_waitcnt lgkmcnt(0)\n\t" \
  "s_nop 1\n\t" \
  "v_smfmac_f32_32x32x32_bf16 %[o], v[108:111], v[88:95], %[idx]\n\t" \
  "v_smfmac_f32_32x32x32_bf16 %[o2], v[108:111], v[144:151], %[idx]\n\t" \
  "s_waitcnt vmcnt(0)\n\t" \
  "s_nop 7\n\t" \
  "s_nop 7\n\t"

#define ATTN_CLOB "v64","v65","v66","v67","v68","v69","v70","v71","v72","v73","v74","v75","v76","v77","v78","v79","v80","v81","v82","v83","v84","v85","v86","v87","v88","v89","v90","v91","v92","v93","v94","v95", \
  "v96","v97","v98","v99","v104","v105","v106","v107","v108","v109","v110","v111", \
  "v120","v121","v122","v123","v124","v125","v126","v127","v128","v129","v130","v131","v132","v133","v134","v135","v136","v137","v138","v139","v140","v141","v142","v143","v144","v145","v146","v147","v148","v149","v150","v151"

__global__ void __launch_bounds__(512, 2) fused_block(Params p) {
    asm volatile("fused_code_begin:");
    extern __shared__ __attribute__((aligned(16))) char lds_g[];
    LAS char* lds = (LAS char*)lds_g;
    char* ws = p.ws;
    const int tid = threadIdx.x, lane = tid & 63;
    const int w = __builtin_amdgcn_readfirstlane(tid >> 6);
    const int b = blockIdx.x;

    if (tid < 64) *(LAS u32*)(lds + L_FLAG + tid * 4) = 0;
    else if (tid < 66) *(LAS u32*)(lds + (tid == 64 ? L_TS + 4 : L_FLAG2)) = 0;
    bar();
    const __amdgpu_buffer_rsrc_t wsr = __builtin_amdgcn_make_buffer_rsrc(ws, 0, WS_END, 0x00020000);
    const int xcd = b & 7, j8 = b >> 3;
    const int h = (xcd >> 2) * 16 + (j8 & 15), t = (xcd & 3) * 2 + (j8 >> 4);
    const int R = xcd * 4 + (j8 >> 3), ct = j8 & 7;
    const int c16 = lane & 15, g4 = lane >> 4, r32 = lane & 31, hh = lane >> 5;

    u32 tag;
    u32x4 xf[2][16];
    const u32 xr0 = c16 * 128 + ((g4 ^ ((c16 >> 1) & 7)) * 16), xr1 = c16 * 128 + (((4 + g4) ^ ((c16 >> 1) & 7)) * 16);
    LAS char* sig = lds + L_FLAG;
    u32x4 wraw[6];
#define WLOAD() do { _Pragma("unroll") for (int it = 0; it < 6; ++it) wraw[it] = __builtin_amdgcn_raw_buffer_load_b128(wsr, WS_WT + h * 49152 + it * 8192 + tid * 16, 0, 16); SB(); } while (0)
    if (__builtin_expect(w < 4, 1)) {
        u32x4 wp[6];
        const u32 ep = *(const GAS u32*)(ws + WS_EPOCH);
        const int bm = b < 192 ? b : b - 64;
        const int mq = bm & 3, ms = bm >> 2;
        u32 cv[4]; u32x4 wm[4];
#define MODLOADS() do { _Pragma("unroll") for (int i = 0; i < 4; ++i) { const int row = mq * 128 + i * 32 + (tid >> 3); cv[i] = p.c[row]; wm[i] = *(const u32x4*)(p.wmod + (size_t)row * 3072 + ms * 64 + (tid & 7) * 8); } SB(); } while (0)
#define RELAYOUT_LOAD() do { _Pragma("unroll") for (int it = 0; it < 6; ++it) { const int line = it * 8 + (lane >> 3); wp[it] = *(const u32x4*)(p.wqkv + (size_t)(j8 * 16 + (line & 15)) * 1536 + (line >> 4) * 512 + xcd * 64 + (lane & 7) * 8); } SB(); } while (0)
#define MODPART() do { float e[8] = {0, 0, 0, 0, 0, 0, 0, 0}; \
        _Pragma("unroll") for (int i = 0; i < 4; ++i) { const float cf = __uint_as_float(cv[i] << 16); const u32 s2 = pk2(silu(cf), 0.f), s3 = s2 << 16; \
            _Pragma("unroll") for (int k = 0; k < 4; ++k) { e[2 * k] = dot2(wm[i][k], s2, e[2 * k]); e[2 * k + 1] = dot2(wm[i][k], s3, e[2 * k + 1]); } } \
        float a4[4], a2[2]; \
        _Pragma("unroll") for (int i = 0; i < 4; ++i) a4[i] = swap32_sum(e[i], e[i + 4]);           \
        _Pragma("unroll") for (int i = 0; i < 2; ++i) a2[i] = swap16_sum(a4[i], a4[i + 2]);         \
        _Pragma("unroll") for (int i = 0; i < 2; ++i) a2[i] = dpp_add<0x128>(a2[i]);                \
        if (c16 < 8) *(LAS f32x2*)(lds + L_RED + w * 256 + (c16 * 8 + (g4 & 1) * 2 + (g4 >> 1) * 4) * 4) = (f32x2){a2[0], a2[1]}; \
        lds_set(sig + w * 4); } while (0)
#define GATHER(DELAY) do { u32x4 g[8]; u32 n = 0; if (DELAY) __builtin_amdgcn_s_sleep(DELAY);        \
        for (;;) { bool ok = true; \
            _Pragma("unroll") for (int i = 0; i < 8; ++i) { g[i] = __builtin_amdgcn_raw_buffer_load_b128(wsr, WS_MODG + ((i >> 1) * 3072 + (w < 2 ? 512 + w * 256 : (w - 2) * 256) + (i & 1) * 128 + 2 * lane) * 8, 0, 16); ok = ok && g[i][1] == tag && g[i][3] == tag; } \
            if (__all(ok)) break; __builtin_amdgcn_s_sleep(8); if (spin(n, ws)) break; } \
        lds_await<1>(sig + 20);         \
        _Pragma("unroll") for (int hf = 0; hf < 2; ++hf) { \
            float v0 = rbf((__uint_as_float(g[hf][0]) + __uint_as_float(g[2 + hf][0])) + (__uint_as_float(g[4 + hf][0]) + __uint_as_float(g[6 + hf][0]))); \
            float v1 = rbf((__uint_as_float(g[hf][2]) + __uint_as_float(g[2 + hf][2])) + (__uint_as_float(g[4 + hf][2]) + __uint_as_float(g[6 + hf][2]))); \
            if (w < 2) { v0 = rbf(1.f + v0); v1 = rbf(1.f + v1); } \
            *(LAS f32x2*)(lds + L_AS + ((w < 2 ? w * 256 : 512 + (w - 2) * 256) + hf * 128 + 2 * lane) * 4) = (f32x2){v0, v1}; } \
        lds_set(sig + 32 + w * 4); } while (0)
#define XFRAGS() do { lds_await<1>(sig + 48 + w * 4); \
        _Pragma("unroll") for (int kc = 0; kc < 8; ++kc) _Pragma("unroll") for (int tt = 0; tt < 2; ++tt) { xf[tt][2 * kc] = *(const LAS u32x4*)(lds + L_XA + w * 32768 + kc * 4096 + tt * 2048 + xr0); xf[tt][2 * kc + 1] = *(const LAS u32x4*)(lds + L_XA + w * 32768 + kc * 4096 + tt * 2048 + xr1); } \
        lds_set(sig + 64 + w * 4); } while (0)
        if (w == 3) {
            __builtin_amdgcn_s_setprio(3);
            RELAYOUT_LOAD();
            MODLOADS();

            tag = ep + 0x9E3779B9u;
            {
                const int r = (lane >> 3);
#pragma unroll
                for (int it = 0; it < 6; ++it) {
                    const int slot = ((it & 1) * 8 + r) ^ ((it & 1) * 4);
                    *(LAS u32x4*)(lds + L_WS + (((lane & 7) >> 1) * 3 + (it >> 1)) * 512 + slot * 32 + (lane & 1) * 16) = wp[it];
                }
#pragma unroll
                for (int it = 0; it < 6; ++it) {
                    const u32x4 v = *(const LAS u32x4*)(lds + L_WS + it * 1024 + lane * 16);
                    const int hs = 2 * it + (lane >> 5), hd = hs / 3, sec = hs - hd * 3;
                    __builtin_amdgcn_raw_buffer_store_b128(v, wsr, WS_WT + (xcd * 4 + hd) * 49152 + sec * 16384 + j8 * 512 + (lane & 31) * 16, 0, 16);
                }
                lds_set(sig + 20);
            }
            MODPART();

            drain();
            put_flag64(ws, WS_WTF + (xcd * 32 + j8) * 64, tag, lane);

            XFRAGS();
            { u32 n = 0; for (;;) { if (__all(get_flag(ws, WS_WTF + ((h >> 2) * 32 + r32) * 64) == tag)) break; __builtin_amdgcn_s_sleep(8); if (spin(n, ws)) break; } }
            cfence();
            lds_set(sig + 16);
            WLOAD();
            GATHER(0);
            __builtin_amdgcn_s_setprio(0);
        } else {
            u32 dumA = 0;
            if (w == 1) {
            u64 pc; int back; u32 len, len2;
            asm volatile("s_getpc_b64 %0\n\tfused_code_mark:\n\ts_mov_b32 %1, fused_code_begin - fused_code_mark\n\ts_mov_b32 %2, fused_code_end - fused_code_begin\n\ts_mov_b32 %3, fused_code_end2 - fused_code_begin"
                         : "=s"(pc), "=s"(back), "=s"(len), "=s"(len2));
            pc += (long long)back; len = len > len2 ? len : len2;
            const u32 share = ((len + 4095) >> 12) << 7;
            const u32 off = j8 * share + lane * 128;
            if ((u32)lane * 128 < share && off < len) asm volatile("global_load_dword %0, %1, %2" : "+v"(dumA) : "v"(off), "s"(pc) : "memory");
        }
            MODLOADS();

            tag = ep + 0x9E3779B9u;
            MODPART();
            asm volatile("" :: "v"(dumA));
            if (w == 0) {
                lds_await<4>(sig);
                float s2 = 0;
#pragma unroll
                for (int i = 0; i < 4; ++i) s2 += *(LAS float*)(lds + L_RED + i * 256 + lane * 4);
                if (b < 192) put_granule(ws, WS_MODG + (mq * 3072 + ms * 64 + lane) * 8, tag, __float_as_uint(s2));
            }

            XFRAGS();
            GATHER(0);
            lds_await<1>(sig + 16);
            WLOAD();
        }

    } else {
        u32 dum1 = 0;
        const u32 ep = *(const GAS u32*)(ws + WS_EPOCH);
        u32x4 xs[2];
#pragma unroll
        for (int i = 0; i < 2; ++i) xs[i] = *(const u32x4*)(p.x + (size_t)(b * 8 + (w - 4) * 2 + i) * D + lane * 8);
        SB();

        {
            const __amdgpu_buffer_rsrc_t xsr = __builtin_amdgcn_make_buffer_rsrc((void*)p.x, 0, S * D * 2, 0x00020000);
            const u32 xv0 = (lane >> 3) * 1024 + (((lane & 7) ^ (lane >> 4)) * 16), xv1 = (lane >> 3) * 1024 + (((lane & 7) ^ (4 + (lane >> 4))) * 16);
            const u32 xso = (t * 256 + (w - 4) * 32) * 1024, xm0 = (u32)(size_t)lds_g + L_XA + (w - 4) * 32768;
#pragma unroll
            for (int kc = 0; kc < 8; ++kc) {
                u32 so_;
                asm volatile("s_mov_b32 m0, %1\n\ts_mov_b32 %0, %2\n\tbuffer_load_dwordx4 %3, %5, %0 offen lds\n\t"
                    "s_add_u32 m0, m0, 1024\n\ts_add_u32 %0, %0, 8192\n\tbuffer_load_dwordx4 %4, %5, %0 offen lds\n\t"
                    "s_add_u32 m0, m0, 1024\n\ts_add_u32 %0, %0, 8192\n\tbuffer_load_dwordx4 %3, %5, %0 offen lds\n\t"
                    "s_add_u32 m0, m0, 1024\n\ts_add_u32 %0, %0, 8192\n\tbuffer_load_dwordx4 %4, %5, %0 offen lds"
                    : "=&s"(so_) : "s"(xm0 + kc * 4096), "s"(xso + kc * 128), "v"(xv0), "v"(xv1), "s"(xsr) : "m0", "scc", "memory");
            }
        }
        u32x4 xl[8][4];
        const u16* xbase = p.x + (size_t)(t * 256 + w * 32 + (lane >> 3)) * D + (lane & 7) * 8;
#pragma unroll
        for (int kc = 0; kc < 8; ++kc)
#pragma unroll
            for (int rg = 0; rg < 4; ++rg) xl[kc][rg] = *(const u32x4*)(xbase + rg * 8 * D + kc * 64);
        SB();
        {
            if (w == 4 && lane < 16) {
                const char* pg = lane == 0 ? (const char*)p.wo : lane == 1 ? (const char*)p.wg : lane == 2 ? (const char*)p.wu : lane == 3 ? (const char*)p.wd : lane == 4 ? (const char*)p.wr :
                                 lane == 5 ? (const char*)p.out : lane == 6 ? (const char*)p.out + (S * D * 2 - 4) : lane == 7 ? (const char*)p.wg + (D * F * 2 - 4) : lane == 8 ? (const char*)p.wu + (D * F * 2 - 4) :
                                 lane == 9 ? (const char*)p.wd + (D * F * 2 - 4) : ws + (u32)(lane - 10) * (2u << 20) + (1u << 20);
                asm volatile("global_load_dword %0, %1, off" : "+v"(dum1) : "v"(pg) : "memory");
            }
        }

        asm volatile("s_waitcnt vmcnt(32)" ::: "memory");
        lds_set(sig + 48 + (w - 4) * 4);
        { const u32 ov = (tid & 15) == 0 ? 0x3f803f80u : 0u; *(LAS u32x4*)(lds + L_ONE + (tid - 256) * 16) = (u32x4){ov, ov, ov, ov}; }
        tag = ep + 0x9E3779B9u;
        {
            float mu[2], rs[2];
#pragma unroll
            for (int r = 0; r < 2; ++r) {
                float s = 0;
#pragma unroll
                for (int i = 0; i < 4; ++i) s = dot2(xs[r][i], 0x3f803f80u, s);
                mu[r] = wave_sum(s) * (1.f / D);
            }
#pragma unroll
            for (int r = 0; r < 2; ++r) {
                float q = 0;
#pragma unroll
                for (int i = 0; i < 4; ++i) { const float d0 = bflo(xs[r][i]) - mu[r], d1 = bfhi(xs[r][i]) - mu[r]; q += d0 * d0; q += d1 * d1; }
                rs[r] = ln_rstd(wave_sum(q) * (1.f / D));
                mu[r] = rbf(mu[r]);
            }
            if (lane < 4) put_granule(ws, WS_STATG + ((b * 8 + (w - 4) * 2) * 2 + lane) * 8, tag, __float_as_uint((lane & 1) ? ((lane & 2) ? rs[1] : rs[0]) : ((lane & 2) ? mu[1] : mu[0])));
        }
        LAS char* xt = lds + L_XT + (w - 4) * 4096;
        const u32 xw = (lane >> 3) * 128;
#pragma unroll
        for (int kc = 0; kc < 8; ++kc) {
#pragma unroll
            for (int rg = 0; rg < 4; ++rg) *(LAS u32x4*)(xt + rg * 1024 + xw + (((lane & 7) ^ ((rg * 4 + (lane >> 4)) & 7)) * 16)) = xl[kc][rg];
#pragma unroll
            for (int tt = 0; tt < 2; ++tt) { xf[tt][2 * kc] = *(const LAS u32x4*)(xt + tt * 2048 + xr0); xf[tt][2 * kc + 1] = *(const LAS u32x4*)(xt + tt * 2048 + xr1); }
            SB();
            if (kc == 4) { lds_await<1>(sig + 16); WLOAD(); }
        }
        drain();
        asm volatile("" :: "v"(dum1));

        asm volatile("fused_code_end2:");
    }
    lds_await<4>(sig + 32);
    {
        float av_[2], sv_[2];
#pragma unroll
        for (int q = 0; q < 2; ++q) { const int slot = q * 256 + (tid >> 1), k = slot ^ ((slot >> 1) & 4); av_[q] = *(const LAS float*)(lds + L_AS + k * 4); sv_[q] = *(const LAS float*)(lds + L_AS + 2048 + k * 4); }
        lds_await<4>(sig + 64);
        if (t < 6) {
            const int sec = t >> 1;
            const u32 msk = ((tid & 1) == (t & 1)) ? 0x3f80u : 0u;
            float ecs[16];
#pragma unroll
            for (int i = 0; i < 16; ++i) ecs[i] = 0;
#pragma unroll
            for (int q = 0; q < 2; ++q) {
                u32x4 rw;
                if (sec == 0) rw = wraw[q]; else if (sec == 1) rw = wraw[2 + q]; else rw = wraw[4 + q];
                const u32 sb_ = msk ? (__float_as_uint(sv_[q]) >> 16) : 0u;
#pragma unroll
                for (int i = 0; i < 4; ++i) {
                    const u32 sc = pk2(av_[q] * bflo(rw[i]), av_[q] * bfhi(rw[i]));
                    ecs[2 * i] = dot2(sc, msk, ecs[2 * i]); ecs[2 * i + 1] = dot2(sc, msk << 16, ecs[2 * i + 1]);
                    ecs[8 + 2 * i] = dot2(rw[i], sb_, ecs[8 + 2 * i]); ecs[8 + 2 * i + 1] = dot2(rw[i], sb_ << 16, ecs[8 + 2 * i + 1]);
                }
            }
            float o[4];
            wave_reduce16(ecs, o);
            if (c16 == 0) *(LAS f32x4*)(lds + L_RED + w * 64 + ((g4 & 1) * 4 + (g4 >> 1) * 8) * 4) = (f32x4){o[0], o[1], o[2], o[3]};
        }
#pragma unroll
        for (int it = 0; it < 6; ++it) {
            const float a = av_[it & 1];
            u32x4 wsc;
#pragma unroll
            for (int i = 0; i < 4; ++i) wsc[i] = pk2(a * bflo(wraw[it][i]), a * bfhi(wraw[it][i]));
            *(LAS u32x4*)(lds + L_WP + it * 8192 + tid * 16) = wsc;
        }

        bar();

        if (t < 6 && w == 7 && lane < 16) {
            float s2 = 0;
#pragma unroll
            for (int i = 0; i < 8; ++i) s2 += *(LAS float*)(lds + L_RED + i * 64 + lane * 4);
            put_granule(ws, WS_CSB + (h * 96 + (lane >> 3) * 48 + t * 8 + (lane & 7)) * 8, tag, __float_as_uint(s2));
        }
    }

    const int trq = (lane & 15) >> 2, trp = lane & 3;
    const LAS char* wp0 = lds + L_WP + (8 * g4 + (trq ^ ((g4 & 1) * 4))) * 32 + trp * 8;
    const LAS char* wp1 = lds + L_WP + (8 * g4 + ((4 + trq) ^ ((g4 & 1) * 4))) * 32 + trp * 8;
    f32x4 ak[2] = {}, av[2] = {}, aq[2] = {};
#define KVSTEP(u) do { const bf16x8 wk = cat(trd(wp0 + 16384 + (u) * 1024), trd(wp1 + 16384 + (u) * 1024)); const bf16x8 wv = cat(trd(wp0 + 32768 + (u) * 1024), trd(wp1 + 32768 + (u) * 1024)); \
        _Pragma("unroll") for (int tt = 0; tt < 2; ++tt) { ak[tt] = MFMA16(wk, asbf(xf[tt][u]), ak[tt]); av[tt] = MFMA16(asbf(xf[tt][u]), wv, av[tt]); } } while (0)
    u32x4 gst; u64 gcs = 0;
    const bool cs_ = w >= 4 && tid - 256 < 96;
    if (w >= 4) gst = __builtin_amdgcn_raw_buffer_load_b128(wsr, WS_STATG + (t * 512 + 2 * (tid - 256)) * 8, 0, 16);
    SB();
#define QSTEP(u) do { const bf16x8 wq = cat(trd(wp0 + (u) * 1024), trd(wp1 + (u) * 1024)); _Pragma("unroll") for (int tt = 0; tt < 2; ++tt) aq[tt] = MFMA16(wq, asbf(xf[tt][u]), aq[tt]); } while (0)
    KVSTEP(0); KVSTEP(1); KVSTEP(2); KVSTEP(3); KVSTEP(4); KVSTEP(5); KVSTEP(6); KVSTEP(7);
    KVSTEP(8); KVSTEP(9); KVSTEP(10); KVSTEP(11);
    SB();
    if (cs_) gcs = get_granule(ws, WS_CSB + (h * 96 + tid - 256) * 8);
    SB();
    KVSTEP(12); KVSTEP(13); KVSTEP(14); KVSTEP(15);

    if (w >= 4) {
        u32 n = 0;
        while (!__all(gst[1] == tag && gst[3] == tag && (!cs_ || (u32)(gcs >> 32) == tag)) && !spin(n, ws)) {
            gst = __builtin_amdgcn_raw_buffer_load_b128(wsr, WS_STATG + (t * 512 + 2 * (tid - 256)) * 8, 0, 16);
            if (cs_) gcs = get_granule(ws, WS_CSB + (h * 96 + tid - 256) * 8);
        }
        *(LAS u32x2*)(lds + L_STAT + (tid - 256) * 8) = (u32x2){gst[0], gst[2]};
        if (cs_) *(LAS u32*)(lds + L_CSB + (tid - 256) * 4) = (u32)gcs;
    }
    bar();

    const LAS float* stat = (const LAS float*)(lds + L_STAT);
    const LAS float* csb = (const LAS float*)(lds + L_CSB);
    {
        const f32x4 cs = *(const LAS f32x4*)(csb + 16 + 4 * g4), bi = *(const LAS f32x4*)(csb + 48 + 16 + 4 * g4);
        const f32x4 csq = *(const LAS f32x4*)(csb + 4 * g4), biq = *(const LAS f32x4*)(csb + 48 + 4 * g4);
        const float csv = csb[32 + c16], biv = csb[48 + 32 + c16];
        constexpr float QS = 0.25f * 1.4426950408889634f;
        f32x2 st[2]; f32x4 sa_[2], sb_[2];
#pragma unroll
        for (int tt = 0; tt < 2; ++tt) {
            st[tt] = *(const LAS f32x2*)(stat + (w * 32 + tt * 16 + c16) * 2);
            sa_[tt] = *(const LAS f32x4*)(stat + (w * 32 + tt * 16 + 4 * g4) * 2); sb_[tt] = *(const LAS f32x4*)(stat + (w * 32 + tt * 16 + 4 * g4 + 2) * 2);
        }
        SB();
#pragma unroll
        for (int tt = 0; tt < 2; ++tt) {
            float o[4];
#pragma unroll
            for (int r = 0; r < 4; ++r) o[r] = st[tt][1] * (ak[tt][r] - st[tt][0] * cs[r]) + bi[r];
            *(LAS u32x2*)(lds + L_KV + w * 1024 + (g4 >> 1) * 512 + (tt * 16 + c16) * 16 + (g4 & 1) * 8) = (u32x2){pk2(o[0], o[1]), pk2(o[2], o[3])};
            o[0] = sa_[tt][1] * (av[tt][0] - sa_[tt][0] * csv) + biv; o[1] = sa_[tt][3] * (av[tt][1] - sa_[tt][2] * csv) + biv;
            o[2] = sb_[tt][1] * (av[tt][2] - sb_[tt][0] * csv) + biv; o[3] = sb_[tt][3] * (av[tt][3] - sb_[tt][2] * csv) + biv;
            *(LAS u32x2*)(lds + L_KV + 8192 + w * 1024 + (2 * tt + (g4 >> 1)) * 256 + c16 * 16 + (g4 & 1) * 8) = (u32x2){pk2(o[0], o[1]), pk2(o[2], o[3])};
        }
        asm volatile("s_waitcnt lgkmcnt(0)" ::: "memory");
        const u32x4 kk = *(const LAS u32x4*)(lds + L_KV + w * 1024 + lane * 16), vv = *(const LAS u32x4*)(lds + L_KV + 8192 + w * 1024 + lane * 16);
        const u32 base = WS_KV + (h * 8 + t) * 16384 + w * 1024 + lane * 16;
        __builtin_amdgcn_raw_buffer_store_b128(kk, wsr, base, 0, 16);
        __builtin_amdgcn_raw_buffer_store_b128(vv, wsr, base + 8192, 0, 16);

        QSTEP(0); QSTEP(1); QSTEP(2); QSTEP(3); QSTEP(4); QSTEP(5); QSTEP(6); QSTEP(7);
        SB();
        drain();
        put_flag(ws, WS_KVF + ((h * 8 + t) * 8 + w) * 256 + lane * 4, tag);
        SB();
        QSTEP(8); QSTEP(9); QSTEP(10); QSTEP(11); QSTEP(12); QSTEP(13); QSTEP(14); QSTEP(15);
#pragma unroll
        for (int tt = 0; tt < 2; ++tt) {
            float o[4];
#pragma unroll
            for (int r = 0; r < 4; ++r) o[r] = QS * (st[tt][1] * (aq[tt][r] - st[tt][0] * csq[r]) + biq[r]);
            *(LAS u32x2*)(lds + L_Q + w * 1024 + (g4 >> 1) * 512 + (tt * 16 + c16) * 16 + (g4 & 1) * 8) = (u32x2){pk2(o[0], o[1]), pk2(o[2], o[3])};
        }
    }
    bar();

    const int qp = w & 3, kh = w >> 2;
    const bf16x8 qf0 = *(const LAS bf16x8*)(lds + L_Q + (2 * qp) * 1024 + lane * 16), qf1 = *(const LAS bf16x8*)(lds + L_Q + (2 * qp + 1) * 1024 + lane * 16);
    const u32 lbase = (u32)(size_t)lds_g;
    const u32 ka0 = lbase + L_KV + kh * 4096 + lane * 16;
    const u32 va0 = r32 < 16 ? lbase + L_KV + 8192 + kh * 4096 + hh * 256 + r32 * 16 : lbase + L_ONE + hh * 256 + (r32 - 16) * 16;
    const u32 vinc = r32 < 16 ? 16384u : 0u, idx = 0xddddddddu;
    f32x16 o = {}, o2 = {};
    if (w >= 4) {
        {
            u32 n = 0;
            __builtin_amdgcn_s_sleep(4);
            for (;;) { if (__all(get_flag(ws, WS_KVF + ((h * 8 + (r32 >> 2)) * 8 + (w & 1) * 4 + (r32 & 3)) * 256) == tag)) break; __builtin_amdgcn_s_sleep(2); if (spin(n, ws)) break; }
        }
        cfence();
        const u32 vo = lane * 16, m0 = (u32)(size_t)lds_g + L_KV + (w - 4) * 4096;
#pragma unroll
        for (int s = 1; s < 8; ++s) {
            u32 so_;
            asm volatile("s_mov_b32 m0, %1\n\ts_mov_b32 %0, %2\n\tbuffer_load_dwordx4 %3, %4, %0 offen sc1 lds\n\t"
                "s_add_u32 m0, m0, 1024\n\ts_add_u32 %0, %0, 1024\n\tbuffer_load_dwordx4 %3, %4, %0 offen sc1 lds\n\t"
                "s_add_u32 m0, m0, 1024\n\ts_add_u32 %0, %0, 1024\n\tbuffer_load_dwordx4 %3, %4, %0 offen sc1 lds\n\t"
                "s_add_u32 m0, m0, 1024\n\ts_add_u32 %0, %0, 1024\n\tbuffer_load_dwordx4 %3, %4, %0 offen sc1 lds"
                : "=&s"(so_) : "s"(m0 + s * 16384), "s"(WS_KV + (h * 8 + ((t + s) & 7)) * 16384 + (w - 4) * 4096), "v"(vo), "s"(wsr) : "m0", "scc", "memory");
        }
    }

    {
        u32 ka = ka0, va = va0; int cnt = 1;
        asm volatile(ATTN_LOOP4 : [o] "+v"(o), [o2] "+v"(o2), [ka] "+v"(ka), [va] "+v"(va), [cnt] "+s"(cnt)
                     : [q] "v"(qf0), [q2] "v"(qf1), [idx] "v"(idx), [vinc] "v"(vinc), [kinc] "s"(16384) : "scc", "memory", ATTN_CLOB);
    }

    if (w >= 4) asm volatile("s_waitcnt vmcnt(20)" ::: "memory");
    else if (w == 3 && b < 128) {
        u64 g; u32 n = 0;
        for (;;) { g = get_granule(ws, WS_MODG + ((lane >> 4) * 3072 + 1024 + b * 16 + c16) * 8); if (__all((u32)(g >> 32) == tag) || spin(n, ws)) break; }
        float v = __uint_as_float((u32)g);
        v = swap16_sum(v, v); v = swap32_sum(v, v);
        const float other = __int_as_float(__builtin_amdgcn_update_dpp(0, __float_as_int(v), 0xB1, 0xf, 0xf, true));
        if (lane < 16 && (lane & 1) == 0) put_granule(ws, WS_MODF + (b * 8 + (lane >> 1)) * 8, tag, pk2(v, other));
    }
    bar();

    const int tt3 = w & 1, nt3 = (w >> 1) & 1, kh3 = w >> 2;
    const int ncol = xcd * 64 + j8 * 2;
    typedef u32 u32x32 __attribute__((ext_vector_type(32)));
    typedef u32 u32x8 __attribute__((ext_vector_type(8)));
    u32x32 wo; u32x8 gu; u32x4 wd;
    const u32x4 xres = *(const u32x4*)(p.x + (size_t)(R * 64 + (tid >> 3)) * D + ct * 64 + (tid & 7) * 8);
    const u16 wrv = p.wr[tid];
    u64 mf0 = get_granule(ws, WS_MODF + tid * 8), mf1 = get_granule(ws, WS_MODF + (512 + tid) * 8);
    SB();
    {
        u32 ka = ka0 + 16384, va = va0 + vinc;
        u32 gofs = (tid * F + (b < 224 ? b : 0) * 8) * 2, dofs = (tid * D + ncol) * 2, wofs = ((tid >> 3) * D + ct * 64 + (tid & 7) * 8) * 2;
        const u32 dofs3 = ((tid < 256 ? 1536 + tid : tid) * D + ncol) * 2;
        asm volatile(ATTN_LOOP7 : [o] "+v"(o), [o2] "+v"(o2), [ka] "+v"(ka), [va] "+v"(va), [dofs] "+v"(dofs), [wofs] "+v"(wofs), "=&{v[152:183]}"(wo), "=&{v[184:191]}"(gu), "=&{v[192:195]}"(wd)
                     : [q] "v"(qf0), [q2] "v"(qf1), [idx] "v"(idx), [vinc] "v"(vinc), [kinc] "s"(16384), [gofs] "v"(gofs), [dofs3] "v"(dofs3),
                       [wg] "s"(p.wg), [wu] "s"(p.wu), [wd] "s"(p.wd), [wo] "s"(p.wo) : "scc", "memory", ATTN_CLOB);
    }

    float of[8], lsum;
    for (int pass = 0;; ++pass) {
        f32x4 kp0, kp1, sd0, sd1; float kpl, sdl;
        if (kh) { sd0 = (f32x4){o[0], o[1], o[2], o[3]}; sd1 = (f32x4){o[4], o[5], o[6], o[7]}; sdl = o[8]; kp0 = (f32x4){o2[0], o2[1], o2[2], o2[3]}; kp1 = (f32x4){o2[4], o2[5], o2[6], o2[7]}; kpl = o2[8]; }
        else { kp0 = (f32x4){o[0], o[1], o[2], o[3]}; kp1 = (f32x4){o[4], o[5], o[6], o[7]}; kpl = o[8]; sd0 = (f32x4){o2[0], o2[1], o2[2], o2[3]}; sd1 = (f32x4){o2[4], o2[5], o2[6], o2[7]}; sdl = o2[8]; }
        *(LAS f32x4*)(lds + L_XCH + w * 2304 + lane * 16) = sd0; *(LAS f32x4*)(lds + L_XCH + w * 2304 + 1024 + lane * 16) = sd1; *(LAS float*)(lds + L_XCH + w * 2304 + 2048 + lane * 4) = sdl;
        bar();
        {
            const f32x4 pa = *(const LAS f32x4*)(lds + L_XCH + (w ^ 4) * 2304 + lane * 16), pb = *(const LAS f32x4*)(lds + L_XCH + (w ^ 4) * 2304 + 1024 + lane * 16);
            lsum = kpl + *(const LAS float*)(lds + L_XCH + (w ^ 4) * 2304 + 2048 + lane * 4);
#pragma unroll
            for (int i = 0; i < 4; ++i) { of[i] = kp0[i] + pa[i]; of[4 + i] = kp1[i] + pb[i]; }
        }

        { auto r = __builtin_amdgcn_permlane32_swap(__float_as_uint(lsum), __float_as_uint(lsum), false, false); lsum = __uint_as_float(r[0]); }
        if (pass) { bar(); break; }
        bool ok = lsum > 8.7e-19f && lsum < 1e36f;
#pragma unroll
        for (int i = 0; i < 8; ++i) ok = ok && __builtin_fabsf(of[i]) < 1e36f;
        if (!__all(ok) && lane == 0) *(LAS u32*)(lds + L_FLAG2) = 1;
        bar();
        if (__builtin_expect(*(const LAS u32*)(lds + L_FLAG2) == 0, 1)) break;
        const bf16x8 qq[2] = {qf0, qf1};
        float mx[2] = {-3.0e38f, -3.0e38f};
        const f32x16 z = {};
        for (int kt = 0; kt < 32; ++kt) {
            const bf16x8 kf = *(const LAS bf16x8*)(lds + L_KV + (kt >> 2) * 16384 + kh * 4096 + (kt & 3) * 1024 + lane * 16);
#pragma unroll
            for (int n = 0; n < 2; ++n) { const f32x16 s = MFMA32(kf, qq[n], z);
#pragma unroll
                for (int i = 0; i < 16; ++i) mx[n] = fmaxf(mx[n], s[i]); }
        }
#pragma unroll
        for (int n = 0; n < 2; ++n) { auto r = __builtin_amdgcn_permlane32_swap(__float_as_uint(mx[n]), __float_as_uint(mx[n]), false, false); mx[n] = fmaxf(__uint_as_float(r[0]), __uint_as_float(r[1])); }
        bar();
        *(LAS float*)(lds + L_XCH + (w * 2) * 256 + lane * 4) = mx[0]; *(LAS float*)(lds + L_XCH + (w * 2 + 1) * 256 + lane * 4) = mx[1];
        bar();
        mx[0] = fmaxf(mx[0], *(const LAS float*)(lds + L_XCH + ((w ^ 4) * 2) * 256 + lane * 4)); mx[1] = fmaxf(mx[1], *(const LAS float*)(lds + L_XCH + ((w ^ 4) * 2 + 1) * 256 + lane * 4));
        bar();
        o = z; o2 = z;
        for (int kt = 0; kt < 32; ++kt) {
            const u32 off = (kt >> 2) * 16384 + (kt & 3) * 1024;
            const bf16x8 kf = *(const LAS bf16x8*)(lds + L_KV + off + kh * 4096 + lane * 16);
            const LAS char* vp = r32 < 16 ? lds + L_KV + off + 8192 + kh * 4096 + hh * 256 + r32 * 16 : lds + L_ONE + hh * 256 + (r32 - 16) * 16;
            const bf16x8 v0 = *(const LAS bf16x8*)vp, v1 = *(const LAS bf16x8*)(vp + 512);
#pragma unroll
            for (int n = 0; n < 2; ++n) {
                f32x16 s = MFMA32(kf, qq[n], z);
#pragma unroll
                for (int i = 0; i < 16; ++i) s[i] = __builtin_amdgcn_exp2f(s[i] - mx[n]);
                const f32x8 lo = __builtin_shufflevector(s, s, 0, 1, 2, 3, 4, 5, 6, 7), hi = __builtin_shufflevector(s, s, 8, 9, 10, 11, 12, 13, 14, 15);
                f32x16 acc = n ? o2 : o;
                acc = __builtin_amdgcn_smfmac_f32_32x32x32_bf16(v0, __builtin_bit_cast(bf16x16, lo), acc, 0xdddddddd, 0, 0);
                acc = __builtin_amdgcn_smfmac_f32_32x32x32_bf16(v1, __builtin_bit_cast(bf16x16, hi), acc, 0xdddddddd, 0, 0);
                if (n) o2 = acc; else o = acc;
            }
        }
    }
    {
        const float inv = __builtin_amdgcn_rcpf(lsum);
        u32 p0 = pk2(of[0] * inv, of[1] * inv), p1 = pk2(of[2] * inv, of[3] * inv), p2 = pk2(of[4] * inv, of[5] * inv), p3 = pk2(of[6] * inv, of[7] * inv);
        auto s0 = __builtin_amdgcn_permlane32_swap(p0, p2, false, false); auto s1 = __builtin_amdgcn_permlane32_swap(p1, p3, false, false);
        const u32x4 ov = {s0[0], s1[0], s0[1], s1[1]};
        const int qt = t * 8 + 2 * qp + kh;
        __builtin_amdgcn_raw_buffer_store_b128(ov, wsr, WS_O + h * 65536 + qt * 1024 + lane * 16, 0, 16);
        drain();
        put_flag64(ws, WS_OF + (qt * 32 + h) * 64, tag, lane);
        SB();
#pragma unroll
        for (int it = 0; it < 8; ++it) { const int k = it * 64 + (tid >> 3); *(LAS u32x4*)(lds + L_WO + k * 128 + (((tid & 7) ^ (((k >> 1) & 1) << 2)) * 16)) = (u32x4){wo[4 * it], wo[4 * it + 1], wo[4 * it + 2], wo[4 * it + 3]}; }
        { const int ks = tid ^ ((tid >> 1) & 4); *(LAS u32x4*)(lds + L_GU + ks * 32) = (u32x4){gu[0], gu[1], gu[2], gu[3]}; *(LAS u32x4*)(lds + L_GU + ks * 32 + 16) = (u32x4){gu[4], gu[5], gu[6], gu[7]}; }

        u32 n = 0;
        while (!__all((u32)(mf0 >> 32) == tag && (u32)(mf1 >> 32) == tag) && !spin(n, ws)) { mf0 = get_granule(ws, WS_MODF + tid * 8); mf1 = get_granule(ws, WS_MODF + (512 + tid) * 8); }
        {
            float m0_ = bflo((u32)mf0), m1_ = bfhi((u32)mf0), m2_ = bflo((u32)mf1), m3_ = bfhi((u32)mf1);
            if (w < 4) { m2_ = rbf(1.f + m2_); m3_ = rbf(1.f + m3_); }
            *(LAS f32x2*)(lds + L_MODT + 2 * tid * 4) = (f32x2){m0_, m1_};
            *(LAS f32x2*)(lds + L_MODT + (1024 + 2 * tid) * 4) = (f32x2){m2_, m3_};
        }
    }
    bar();
    {
        const float wr = bf2f(wrv), gk = *(const LAS float*)(lds + L_MODT + (1024 + tid) * 4) * wr;
        const float GS = wave_sum(gk), BS = wave_sum(*(const LAS float*)(lds + L_MODT + (512 + tid) * 4) * wr);
        *(LAS float*)(lds + L_GK + tid * 4) = gk;
        if (lane == 0) *(LAS f32x2*)(lds + L_T + 3072 + w * 8) = (f32x2){GS, BS};
    }

    {
        { u32 n = 0; for (;;) { if (__all(get_flag(ws, WS_OF + ((R * 2 + (lane & 1)) * 32 + w * 4 + ((lane >> 1) & 3)) * 64) == tag)) break; __builtin_amdgcn_s_sleep(2); if (spin(n, ws)) break; } }
        cfence();
        const u32 vo = lane * 16, m0 = (u32)(size_t)lds_g + L_OI + w * 8192;
#pragma unroll
        for (int i = 0; i < 4; ++i) {
            u32 so_;
            asm volatile("s_mov_b32 m0, %1\n\ts_mov_b32 %0, %2\n\tbuffer_load_dwordx4 %3, %4, %0 offen sc1 lds\n\t"
                "s_add_u32 m0, m0, 1024\n\ts_add_u32 %0, %0, 1024\n\tbuffer_load_dwordx4 %3, %4, %0 offen sc1 lds"
                : "=&s"(so_) : "s"(m0 + i * 2048), "s"(WS_O + (w * 4 + i) * 65536 + R * 2048), "v"(vo), "s"(wsr) : "m0", "scc", "memory");
        }
        drain();
        bar();
    }

    float GS = 0, BS = 0;
#pragma unroll
    for (int i = 0; i < 8; ++i) { const f32x2 q = *(const LAS f32x2*)(lds + L_T + 3072 + i * 8); GS += q[0]; BS += q[1]; }
    const LAS float* modt = (const LAS float*)(lds + L_MODT);
    const int n0 = ct * 64 + nt3 * 32 + kh3 * 16 + hh * 4;
    float x2[8];
    {
        f32x16 acc = {};
        const int blk = (lane >> 4) & 1;
        bf16x8 fa[16], fb[16];
#define OPRD(s) do { const int hd = kh3 * 16 + (s); const int k0 = hd * 16 + 8 * hh + trq, k1 = k0 + 4; \
            fa[s] = cat(trd(lds + L_WO + k0 * 128 + ((nt3 * 64 + blk * 32 + trp * 8) ^ (((k0 >> 1) & 1) << 6))), trd(lds + L_WO + k1 * 128 + ((nt3 * 64 + blk * 32 + trp * 8) ^ (((k1 >> 1) & 1) << 6)))); \
            fb[s] = *(const LAS bf16x8*)(lds + L_OI + hd * 2048 + tt3 * 1024 + lane * 16); } while (0)
        OPRD(0); OPRD(1); OPRD(2); OPRD(3);
#pragma unroll
        for (int g = 0; g < 4; ++g) {
            SB();
            if (g < 3) { OPRD(4 * g + 4); OPRD(4 * g + 5); OPRD(4 * g + 6); OPRD(4 * g + 7); }
            SB();
#pragma unroll
            for (int s = 4 * g; s < 4 * g + 4; ++s) acc = MFMA32(fa[s], fb[s], acc);
        }

        f32x4 kp0, kp1, sd0, sd1;
        if (kh3) { sd0 = (f32x4){acc[0], acc[1], acc[2], acc[3]}; sd1 = (f32x4){acc[4], acc[5], acc[6], acc[7]}; kp0 = (f32x4){acc[8], acc[9], acc[10], acc[11]}; kp1 = (f32x4){acc[12], acc[13], acc[14], acc[15]}; }
        else { kp0 = (f32x4){acc[0], acc[1], acc[2], acc[3]}; kp1 = (f32x4){acc[4], acc[5], acc[6], acc[7]}; sd0 = (f32x4){acc[8], acc[9], acc[10], acc[11]}; sd1 = (f32x4){acc[12], acc[13], acc[14], acc[15]}; }
        bar();

        *(LAS f32x4*)(lds + L_EX + w * 2048 + lane * 16) = sd0; *(LAS f32x4*)(lds + L_EX + w * 2048 + 1024 + lane * 16) = sd1;
        *(LAS u32x4*)(lds + L_XR + (tid >> 3) * 144 + (tid & 7) * 16) = xres;
        bar();

        const LAS char* xrp = lds + L_XR + (tt3 * 32 + r32) * 144 + (nt3 * 32 + kh3 * 16 + hh * 4) * 2;
        const u32x2 xa = *(const LAS u32x2*)xrp, xb = *(const LAS u32x2*)(xrp + 16);
        const f32x4 pa = *(const LAS f32x4*)(lds + L_EX + (w ^ 4) * 2048 + lane * 16), pb = *(const LAS f32x4*)(lds + L_EX + (w ^ 4) * 2048 + 1024 + lane * 16);
        const f32x4 ga0 = *(const LAS f32x4*)(modt + n0), ga1 = *(const LAS f32x4*)(modt + n0 + 8);
        const f32x4 gk0 = *(const LAS f32x4*)(lds + L_GK + n0 * 4), gk1 = *(const LAS f32x4*)(lds + L_GK + (n0 + 8) * 4);
        SB();
        x2[0] = bflo(xa[0]) + ga0[0] * (kp0[0] + pa[0]); x2[1] = bfhi(xa[0]) + ga0[1] * (kp0[1] + pa[1]); x2[2] = bflo(xa[1]) + ga0[2] * (kp0[2] + pa[2]); x2[3] = bfhi(xa[1]) + ga0[3] * (kp0[3] + pa[3]);
        x2[4] = bflo(xb[0]) + ga1[0] * (kp1[0] + pb[0]); x2[5] = bfhi(xb[0]) + ga1[1] * (kp1[1] + pb[1]); x2[6] = bflo(xb[1]) + ga1[2] * (kp1[2] + pb[2]); x2[7] = bfhi(xb[1]) + ga1[3] * (kp1[3] + pb[3]);
        float s1 = ((x2[0] + x2[1]) + (x2[2] + x2[3])) + ((x2[4] + x2[5]) + (x2[6] + x2[7]));
        float s2 = 0, s3 = 0;
#pragma unroll
        for (int i = 0; i < 8; ++i) { s2 += x2[i] * x2[i]; s3 += x2[i] * (i < 4 ? gk0[i & 3] : gk1[i & 3]); }
        s1 = swap32_sum(s1, s1); s2 = swap32_sum(s2, s2); s3 = swap32_sum(s3, s3);
        if (hh == 0) { LAS float* q = (LAS float*)(lds + L_T + ((nt3 * 2 + kh3) * 64 + tt3 * 32 + r32) * 12); q[0] = s1; q[1] = s2; q[2] = s3; }

        bar();

        if (tid < 192) {
            const int row = tid & 63, j = tid >> 6;
            float v = 0;
#pragma unroll
            for (int i = 0; i < 4; ++i) v += *(const LAS float*)(lds + L_T + (i * 64 + row) * 12 + j * 4);
            put_granule(ws, WS_PST + ((R * 8 + ct) * 192 + tid) * 8, tag, __float_as_uint(v));
        }
    }

    {
        if (w < 3) {
            u64 g[8]; u32 n = 0;
            __builtin_amdgcn_s_sleep(6);
            for (;;) {
                bool ok = true;
#pragma unroll
                for (int i = 0; i < 8; ++i) { g[i] = get_granule(ws, WS_PST + ((R * 8 + i) * 192 + w * 64 + lane) * 8); ok = ok && (u32)(g[i] >> 32) == tag; }
                if (__all(ok) || spin(n, ws)) break;
            }
            float v = 0;
#pragma unroll
            for (int i = 0; i < 8; ++i) v += __uint_as_float((u32)g[i]);
            *(LAS float*)(lds + L_PS + w * 256 + lane * 4) = v;
        }
        bar();
    }

    u32x4 outv;
    {
        const LAS float* ps = (const LAS float*)(lds + L_PS);
        const float mu_l = ps[lane] * (1.f / D);
        const float rs_l = ln_rstd(fmaxf(ps[64 + lane] * (1.f / D) - mu_l * mu_l, 0.f));
        const u32 bits = __float_as_uint(rs_l * (ps[128 + lane] - rbf(mu_l) * GS) + BS);
        u32 key = ((bits ^ ((bits >> 31) ? 0xffffffffu : 0x80000000u)) & ~63u) | (63 - lane);
        const u32 k1 = wave_max(key);
        const u32 k2 = wave_max(key == k1 ? 0u : key);
        const int c1 = 63 - (int)(k1 & 63), c2 = 63 - (int)(k2 & 63), myrow = tt3 * 32 + r32;
        if (ct == 0 && tid < 8) {
            u32 val = 0;
            if (tid < 2) val = tid ? k2 : k1;
            else if (tid < 6) { const int cr = tid < 4 ? c1 : c2; const float m_ = ps[cr] * (1.f / D);
                val = __float_as_uint((tid & 1) ? ln_rstd(fmaxf(ps[64 + cr] * (1.f / D) - m_ * m_, 0.f)) : rbf(m_)); }
            put_granule(ws, WS_CAND + (R * 8 + tid) * 8, tag, val);
        }
        if (myrow == c1 || myrow == c2) {
            LAS float* q = (LAS float*)(lds + L_T + 1024 + (myrow == c2 ? 256 : 0)) + nt3 * 32 + kh3 * 16 + hh * 4;
            *(LAS f32x4*)q = (f32x4){x2[0], x2[1], x2[2], x2[3]}; *(LAS f32x4*)(q + 8) = (f32x4){x2[4], x2[5], x2[6], x2[7]};
        }
        bar();
        if (tid < 128) put_granule(ws, WS_X2G + ((R * 2 + (tid >> 6)) * 512 + ct * 64 + (tid & 63)) * 8, tag, *(const LAS u32*)(lds + L_T + 1024 + tid * 4));
        const float mu_r = ps[myrow] * (1.f / D);
        const f32x2 st = {rbf(mu_r), ln_rstd(fmaxf(ps[64 + myrow] * (1.f / D) - mu_r * mu_r, 0.f))};
        float ov[8];
#pragma unroll
        for (int i = 0; i < 8; ++i) { const int n = n0 + (i >> 2) * 8 + (i & 3);
            const float h2 = (x2[i] - st[0]) * st[1] * modt[1024 + n] + modt[512 + n];
            ov[i] = x2[i] + modt[1536 + n] * h2; }
        const u32 p0 = pk2(ov[0], ov[1]), p1 = pk2(ov[2], ov[3]), p2 = pk2(ov[4], ov[5]), p3 = pk2(ov[6], ov[7]);
        auto s0 = __builtin_amdgcn_permlane32_swap(p0, p2, false, false); auto s1 = __builtin_amdgcn_permlane32_swap(p1, p3, false, false);
        outv = (u32x4){s0[0], s1[0], s0[1], s1[1]};
    }

    int rowA, rowB, slotA, slotB; float rA, rB;
    {
        if (w == 0) {
            u64 g1; u32 n = 0;
            __builtin_amdgcn_s_sleep(10);
            for (;;) { g1 = get_granule(ws, WS_CAND + ((lane >> 1) * 8 + (lane & 1)) * 8); if (__all((u32)(g1 >> 32) == tag) || spin(n, ws)) break; }
            const u32 key = (u32)g1;
            const u32 kA = wave_max(key);
            const int sA = __builtin_ctzll(__ballot(key == kA));
            const u32 kB = wave_max(lane == sA ? 0u : key);
            const int sB = __builtin_ctzll(__ballot(key == kB && lane != sA));
            if (lane == 0) *(LAS u32x4*)(lds + L_TS + 16) = (u32x4){(u32)sA, (u32)sB, kA, kB};
            lds_set(lds + L_TS + 4);
        } else lds_await<1>(lds + L_TS + 4);
        const u32x4 sel = *(const LAS u32x4*)(lds + L_TS + 16);
        slotA = __builtin_amdgcn_readfirstlane(sel[0]) & 63; slotB = __builtin_amdgcn_readfirstlane(sel[1]) & 63;
        const u32 kA = __builtin_amdgcn_readfirstlane(sel[2]), kB = __builtin_amdgcn_readfirstlane(sel[3]);
        rowA = (slotA >> 1) * 64 + (63 - (int)(kA & 63)); rowB = (slotB >> 1) * 64 + (63 - (int)(kB & 63));
        const u32 a_ = kA & ~63u, b_ = kB & ~63u;
        rA = __uint_as_float((a_ >> 31) ? a_ ^ 0x80000000u : ~a_ & ~63u); rB = __uint_as_float((b_ >> 31) ? b_ ^ 0x80000000u : ~b_ & ~63u);
    }

    {
        const int tk = tid >> 8, cp = (tid & 255) * 2, slot = tk ? slotB : slotA;
        u32x4 gx; u64 gm, gr; u32 n = 0;
        for (;;) {
            gx = __builtin_amdgcn_raw_buffer_load_b128(wsr, WS_X2G + (slot * 512 + cp) * 8, 0, 16);
            gm = get_granule(ws, WS_CAND + ((slot >> 1) * 8 + 2 + (slot & 1) * 2) * 8); gr = get_granule(ws, WS_CAND + ((slot >> 1) * 8 + 3 + (slot & 1) * 2) * 8);
            if (__all(gx[1] == tag && gx[3] == tag && (u32)(gm >> 32) == tag && (u32)(gr >> 32) == tag) || spin(n, ws)) break;
        }
        const float mu = __uint_as_float((u32)gm), rs = __uint_as_float((u32)gr), xa = __uint_as_float(gx[0]), xb = __uint_as_float(gx[2]);
        const float ha = (xa - mu) * rs * modt[1024 + cp] + modt[512 + cp], hb = (xb - mu) * rs * modt[1024 + cp + 1] + modt[512 + cp + 1];
        *(LAS u32*)(lds + L_H2 + (tk * 512 + cp) * 2) = pk2(ha, hb);
        *(LAS f32x4*)(lds + L_X2S + (tk * 256 + (tid & 255)) * 16) = (f32x4){xa, xb, ha, hb};

        bar();

    }
    if (b < 224) {
        f32x4 acc = {};
        const LAS char* gp0 = lds + L_GU + (w * 64 + 8 * g4 + (trq ^ ((g4 & 1) * 4))) * 32 + trp * 8;
        const LAS char* gp1 = lds + L_GU + (w * 64 + 8 * g4 + ((4 + trq) ^ ((g4 & 1) * 4))) * 32 + trp * 8;
#pragma unroll
        for (int uu = 0; uu < 2; ++uu) {
            u32x4 af = *(const LAS u32x4*)(lds + L_H2 + ((c16 & 1) * 512 + w * 64 + uu * 32 + g4 * 8) * 2);
            if (c16 >= 2) af = (u32x4){0, 0, 0, 0};
            acc = MFMA16(asbf(af), cat(trd(gp0 + uu * 1024), trd(gp1 + uu * 1024)), acc);
        }
        if (lane < 16) { *(LAS float*)(lds + L_T + 1024 + (w * 32 + lane) * 4) = acc[0]; *(LAS float*)(lds + L_T + 1024 + (w * 32 + 16 + lane) * 4) = acc[1]; }
        bar();
        if (tid < 16) {
            const int tk = tid >> 3, f = tid & 7;
            float g = 0, u = 0;
#pragma unroll
            for (int i = 0; i < 8; ++i) { g += *(const LAS float*)(lds + L_T + 1024 + (i * 32 + tk * 16 + f) * 4); u += *(const LAS float*)(lds + L_T + 1024 + (i * 32 + tk * 16 + 8 + f) * 4); }
            const float a = rbf(silu(rbf(g))) * rbf(u);
            const float other = __int_as_float(__builtin_amdgcn_update_dpp(0, __float_as_int(a), 0x128, 0xf, 0xf, true));
            if (tid < 8) put_granule(ws, WS_ACT + (b * 8 + f) * 8, tag, pk2(a, other));
        }
    }

    {
        {
            u64 g[4]; u32 n = 0;
            __builtin_amdgcn_s_sleep(12);
            for (;;) {
                bool ok = true;
#pragma unroll
                for (int i = 0; i < 4; ++i) { const int f = (i < 3 || tid < 256) ? i * 512 + tid : tid; g[i] = get_granule(ws, WS_ACT + f * 8); ok = ok && (u32)(g[i] >> 32) == tag; }
                if (__all(ok) || spin(n, ws)) break;
            }

            float y[4] = {0, 0, 0, 0};
#pragma unroll
            for (int i = 0; i < 4; ++i) {
                const u32 av = (i < 3 || tid < 256) ? (u32)g[i] : 0u;
                const float aA = bflo(av), aB = bfhi(av), w0 = bflo(wd[i]), w1 = bfhi(wd[i]);
                y[0] += aA * w0; y[1] += aA * w1; y[2] += aB * w0; y[3] += aB * w1;
            }
#pragma unroll
            for (int i = 0; i < 4; ++i) y[i] = wave_sum(y[i]);
            if (lane == 0) *(LAS f32x4*)(lds + L_T + 2048 + w * 16) = (f32x4){y[0], y[1], y[2], y[3]};
        }
        bar();
    {
        const int grow = R * 64 + tt3 * 32 + r32;
        if (grow != rowA && grow != rowB) *(u32x4*)(p.out + (size_t)grow * D + ct * 64 + nt3 * 32 + kh3 * 16 + hh * 8) = outv;
    }
        if (tid < 4) {
            float yy = 0;
#pragma unroll
            for (int i = 0; i < 8; ++i) yy += *(const LAS float*)(lds + L_T + 2048 + i * 16 + tid * 4);
            const int tk = tid >> 1, n_ = ncol + (tid & 1), row = tk ? rowB : rowA;
            const float rr = tk ? rB : rA;
            const float sg = __builtin_amdgcn_rcpf(1.f + __expf(-rr));
            const f32x4 xh = *(const LAS f32x4*)(lds + L_X2S + (tk * 256 + (ncol >> 1)) * 16);
            const float ov = xh[tid & 1] + modt[1536 + n_] * (xh[2 + (tid & 1)] + yy * sg);
            const float other = __int_as_float(__builtin_amdgcn_update_dpp(0, __float_as_int(ov), 0xB1, 0xf, 0xf, true));
            if ((tid & 1) == 0) *(u32*)(p.out + (size_t)row * D + ncol) = pk2(ov, other);
        }
        if (b == 0 && tid == 0) put_flag(ws, WS_EPOCH, tag);
    }
    asm volatile("fused_code_end:");

}

extern "C" void kernel_launch(void* const* d_in, const int* in_sizes, int n_in, void* d_out, int out_size, void* d_ws, size_t ws_size, hipStream_t stream) {
    Params p;
    p.x = (const u16*)d_in[0]; p.wqkv = (const u16*)d_in[1]; p.wo = (const u16*)d_in[2];
    p.wr = (const u16*)d_in[4]; p.wg = (const u16*)d_in[5]; p.wu = (const u16*)d_in[6]; p.wd = (const u16*)d_in[7];
    p.c = (const u16*)d_in[8]; p.wmod = (const u16*)d_in[9];
    p.out = (u16*)d_out; p.ws = (char*)d_ws;
    (void)hipFuncSetAttribute((const void*)fused_block, hipFuncAttributeMaxDynamicSharedMemorySize, L_END);
    fused_block<<<256, 512, L_END, stream>>>(p);
}
```
